```python
import torch

def _softmax(x, axis=-1):
    return torch.softmax(x, dim=axis)
def _gelu(x):
    return torch.nn.functional.gelu(x, approximate="tanh")
def _silu(x):
    return torch.nn.functional.silu(x)
def _sigmoid(x):
    return torch.sigmoid(x)
def _layernorm(x, axis=-1, eps=1e-5):
    m = x.mean(dim=axis, keepdim=True)
    v = x.var(dim=axis, keepdim=True, unbiased=False)
    return (x - m) / torch.sqrt(v + eps)
def _scatter_add(tgt, idx, src, axis=0):
    return tgt.clone().index_add_(axis, idx, src)
def _segment_sum(src, seg, n):
    out = torch.zeros((n,) + src.shape[1:], dtype=src.dtype)
    return out.index_add_(0, seg, src)
def _pad(x, pw):
    flat = tuple(v for pair in reversed(pw) for v in pair)
    return torch.nn.functional.pad(x, flat)

kernel_name = 'sequential_preLN__spectral_gfnet__glu_gated_geglu__prompt'

def setup_inputs():

    _w = (lambda a: (lambda t: t.to(torch.bfloat16) if t.is_floating_point() else t)(torch.as_tensor(a)))
    def _A_setup():
        B, S, D = 1, 2048, 512
        Df = D // 2 + 1
        return {
            "x": _w((lambda *s: torch.randn(*s, dtype=torch.bfloat16) * 0.05)(B, S, D)),
            "w_r": _w((lambda *s: torch.randn(*s, dtype=torch.bfloat16) * 0.4)(S, Df) * 0.02),
            "w_i": _w((lambda *s: torch.randn(*s, dtype=torch.bfloat16) * 0.4)(S, Df) * 0.02),
        }
    _A_in = _A_setup()

    def _B_setup():
        B, S, Dm, Df = 1, 2048, 512, 1376
        return {
            "x": _w((lambda *s: torch.randn(*s, dtype=torch.bfloat16) * 0.05)(B, S, Dm)),
            "w_gate": _w((lambda *s: torch.randn(*s, dtype=torch.bfloat16) * 0.4)(Dm, Df) * (Dm ** -0.5)),
            "w_up": _w((lambda *s: torch.randn(*s, dtype=torch.bfloat16) * 0.4)(Dm, Df) * (Dm ** -0.5)),
            "w_down": _w((lambda *s: torch.randn(*s, dtype=torch.bfloat16) * 0.4)(Df, Dm) * (Df ** -0.5)),
        }
    _B_in = _B_setup()

    return {
        "A_x": _A_in["x"],
        "A_w_r": _A_in["w_r"],
        "A_w_i": _A_in["w_i"],
        "B_x": _B_in["x"],
        "B_w_gate": _B_in["w_gate"],
        "B_w_up": _B_in["w_up"],
        "B_w_down": _B_in["w_down"],
    }

def reference(A_x, A_w_r, A_w_i, B_x, B_w_gate, B_w_up, B_w_down):
    def _A_ref(x, w_r, w_i):
        B, S, D = x.shape
        X = (lambda x, axes=(-2,-1): torch.fft.rfft2(x.float(), dim=axes))(x, axes=(-2, -1))
        W = w_r + 1j * w_i
        X = X * W
        y = (lambda x, s, axes=(-2,-1): torch.fft.irfft2(x, s=s, dim=axes).to(torch.bfloat16))(X, (S, D), axes=(-2, -1))
        return y

    def _B_ref(x, w_gate, w_up, w_down):
        g = torch.einsum("bsd,df->bsf", x, w_gate)
        u = torch.einsum("bsd,df->bsf", x, w_up)
        h = _gelu(g) * u
        return torch.einsum("bsf,fd->bsd", h, w_down)

    x = A_x
    h = x + _A_ref(_layernorm(x, axis=-1), A_w_r, A_w_i)
    h2 = h + _B_ref(_layernorm(h, axis=-1), B_w_gate, B_w_up, B_w_down)
    return h2
```

```cpp
#include <hip/hip_runtime.h>
#include <hip/hip_bf16.h>
#include <stdint.h>

constexpr int S = 2048, D = 512, DF = 257, F = 1376;

__device__ __forceinline__ float bf2f(uint16_t v) { return __uint_as_float(((uint32_t)v) << 16); }
__device__ __forceinline__ uint16_t f2bf(float f) { uint32_t u = __float_as_uint(f); u += 0x7FFF + ((u >> 16) & 1); return (uint16_t)(u >> 16); }
__device__ __forceinline__ float rbf(float f) { return bf2f(f2bf(f)); }

__device__ float block_sum(float v, float* red) {
  for (int o = 32; o > 0; o >>= 1) v += __shfl_xor(v, o);
  __syncthreads();
  if ((threadIdx.x & 63) == 0) red[threadIdx.x >> 6] = v;
  __syncthreads();
  float s = 0; for (int i = 0; i < (int)(blockDim.x >> 6); ++i) s += red[i];
  return s;
}

__global__ void k1(const uint16_t* x, float2* X1) {
  __shared__ float n[D]; __shared__ float red[8]; __shared__ float ct[D], st[D];
  int s = blockIdx.x, t = threadIdx.x;
  float v = bf2f(x[s * D + t]);
  float m = block_sum(v, red) / D;
  float var = block_sum((v - m) * (v - m), red) / D;
  float mb = rbf(m), vb = rbf(var);
  float sd = rbf(sqrtf(rbf(vb + 1e-5f)));
  n[t] = rbf(rbf(v - mb) / sd);
  ct[t] = cospif(2.0f * t / D); st[t] = sinpif(2.0f * t / D);
  __syncthreads();
  if (t < DF) {
    float re = 0, im = 0;
    for (int d = 0; d < D; ++d) { int i = (d * t) & (D - 1); re += n[d] * ct[i]; im -= n[d] * st[i]; }
    X1[t * S + s] = make_float2(re, im);
  }
}
__global__ void k2(const float2* X1, const uint16_t* wr, const uint16_t* wi, float2* Z) {
  __shared__ float2 a[S]; __shared__ float2 b[S]; __shared__ float2 tw[S];
  int k = blockIdx.x, t = threadIdx.x;
  for (int i = t; i < S; i += 1024) { a[i] = X1[k * S + i]; tw[i] = make_float2(cospif(2.0f * i / S), sinpif(2.0f * i / S)); }
  __syncthreads();
  for (int j = t; j < S; j += 1024) {
    float re = 0, im = 0;
    for (int s = 0; s < S; ++s) { float2 w = tw[(s * j) & (S - 1)]; float2 v = a[s]; re += v.x * w.x + v.y * w.y; im += v.y * w.x - v.x * w.y; }
    float c = bf2f(wr[j * DF + k]), d = bf2f(wi[j * DF + k]);
    b[j] = make_float2(re * c - im * d, re * d + im * c);
  }
  __syncthreads();
  for (int j = t; j < S; j += 1024) {
    float re = 0, im = 0;
    for (int s = 0; s < S; ++s) { float2 w = tw[(s * j) & (S - 1)]; float2 v = b[s]; re += v.x * w.x - v.y * w.y; im += v.y * w.x + v.x * w.y; }
    Z[j * DF + k] = make_float2(re, im);
  }
}
__global__ void k3(const uint16_t* x, const float2* Z, uint16_t* h, uint16_t* hn) {
  __shared__ float2 z[DF]; __shared__ float red[8]; __shared__ float ct[D], st[D];
  int s = blockIdx.x, t = threadIdx.x;
  if (t < DF) z[t] = Z[s * DF + t];
  ct[t] = cospif(2.0f * t / D); st[t] = sinpif(2.0f * t / D);
  __syncthreads();
  float acc = 0;
  for (int k = 0; k < DF; ++k) { int i = (k * t) & (D - 1); float c = (k == 0 || k == 256) ? 1.f : 2.f; acc += c * (z[k].x * ct[i] - z[k].y * st[i]); }
  float y = rbf(acc * (1.0f / (S * D)));
  float v = rbf(bf2f(x[s * D + t]) + y);
  h[s * D + t] = f2bf(v);
  float m = block_sum(v, red) / D;
  float var = block_sum((v - m) * (v - m), red) / D;
  float mb = rbf(m), vb = rbf(var);
  float sd = rbf(sqrtf(rbf(vb + 1e-5f)));
  hn[s * D + t] = f2bf(rbf(v - mb) / sd);
}
__global__ void k4(const uint16_t* hn, const uint16_t* wg, const uint16_t* wu, uint16_t* act) {
  __shared__ float a[D];
  int s = blockIdx.x, t = threadIdx.x;
  a[t] = bf2f(hn[s * D + t]);
  __syncthreads();
  for (int f = t; f < F; f += 512) {
    float g = 0, u = 0;
    for (int d = 0; d < D; ++d) { g += a[d] * bf2f(wg[d * F + f]); u += a[d] * bf2f(wu[d * F + f]); }
    g = rbf(g); u = rbf(u);
    float ge = rbf(0.5f * g * (1.f + tanhf(0.7978845608028654f * (g + 0.044715f * g * g * g))));
    act[s * F + f] = f2bf(ge * u);
  }
}
__global__ void k5(const uint16_t* act, const uint16_t* wd, const uint16_t* h, uint16_t* out) {
  __shared__ float a[F];
  int s = blockIdx.x, t = threadIdx.x;
  for (int f = t; f < F; f += 512) a[f] = bf2f(act[s * F + f]);
  __syncthreads();
  float acc = 0;
  for (int f = 0; f < F; ++f) acc += a[f] * bf2f(wd[f * D + t]);
  out[s * D + t] = f2bf(bf2f(h[s * D + t]) + rbf(acc));
}

extern "C" void kernel_launch(void* const* d_in, const int* in_sizes, int n_in, void* d_out, int out_size, void* d_ws, size_t ws_size, hipStream_t stream) {
  const uint16_t* x = (const uint16_t*)d_in[0];
  const uint16_t* wr = (const uint16_t*)d_in[1];
  const uint16_t* wi = (const uint16_t*)d_in[2];
  const uint16_t* wg = (const uint16_t*)d_in[4];
  const uint16_t* wu = (const uint16_t*)d_in[5];
  const uint16_t* wd = (const uint16_t*)d_in[6];
  char* ws = (char*)d_ws;
  float2* X1 = (float2*)ws; float2* Z = (float2*)(ws + (8 << 20));
  uint16_t* h = (uint16_t*)(ws + (16 << 20)); uint16_t* hn = (uint16_t*)(ws + (20 << 20)); uint16_t* act = (uint16_t*)(ws + (24 << 20));
  k1<<<S, 512, 0, stream>>>(x, X1);
  k2<<<DF, 1024, 0, stream>>>(X1, wr, wi, Z);
  k3<<<S, 512, 0, stream>>>(x, Z, h, hn);
  k4<<<S, 512, 0, stream>>>(hn, wg, wu, act);
  k5<<<S, 512, 0, stream>>>(act, wd, h, (uint16_t*)d_out);
}
```

```cpp
#include <hip/hip_runtime.h>
#include <stdint.h>

typedef unsigned u32;
typedef unsigned long long u64;
typedef u32 v4u __attribute__((ext_vector_type(4)));
typedef u32 v2u __attribute__((ext_vector_type(2)));
typedef float f4 __attribute__((ext_vector_type(4)));
typedef float f2 __attribute__((ext_vector_type(2)));
typedef _Float16 h8 __attribute__((ext_vector_type(8)));
typedef _Float16 h2 __attribute__((ext_vector_type(2)));
typedef __bf16 b8 __attribute__((ext_vector_type(8)));
typedef __bf16 b2 __attribute__((ext_vector_type(2)));
typedef short s4 __attribute__((ext_vector_type(4)));
#define LDSP __attribute__((address_space(3)))
#define DI __device__ __forceinline__
template <class T, class S> __device__ __forceinline__ T bcast(S v) { return __builtin_bit_cast(T, v); }
#define BC(T, x) bcast<T>(x)

constexpr u32 OFF_E = 0;
constexpr u32 OFF_V9 = 4096;
constexpr u32 OFF_U = 65536;
constexpr u32 OFF_V = OFF_U + (2u << 20);
constexpr u32 OFF_HN = OFF_V + (2u << 20);
constexpr u32 OFF_H = OFF_HN + (2u << 20);
constexpr u32 OFF_W2 = OFF_H + (2u << 20);
constexpr u32 OFF_ACT = OFF_W2 + (1u << 20);
constexpr u32 OFF_TS = OFF_ACT + (6u << 20);
constexpr u32 R0 = 0, R1 = 65536, R2 = 131072;
constexpr u32 BUFA = R2, BUFB = R2 + 10240, BUFC = R2 + 20480;
constexpr u32 SPIN_CAP = 200000;

DI f4 mf16(v4u a, v4u b, f4 c) { return __builtin_amdgcn_mfma_f32_16x16x32_f16(BC(h8, a), BC(h8, b), c, 0, 0, 0); }
DI f4 mb16(v4u a, v4u b, f4 c) { return __builtin_amdgcn_mfma_f32_16x16x32_bf16(BC(b8, a), BC(b8, b), c, 0, 0, 0); }
DI u32 pkh(float a, float b) { return BC(u32, __builtin_convertvector((f2{a, b}), h2)); }
DI u32 pkb(float a, float b) { return BC(u32, __builtin_convertvector((f2{a, b}), b2)); }
DI float blo(u32 u) { return BC(float, u << 16); }
DI float bhi(u32 u) { return BC(float, u & 0xffff0000u); }
DI float rbf(float x) { return blo(pkb(x, x)); }
DI float cosr(float r) { return __builtin_amdgcn_cosf(r); }
DI float sinr(float r) { return __builtin_amdgcn_sinf(r); }
DI float dot2(u32 a, u32 b, float c) { return __builtin_amdgcn_fdot2_f32_bf16(BC(b2, a), BC(b2, b), c, false); }
template <int C> DI float dppadd(float x) { return x + BC(float, __builtin_amdgcn_update_dpp(0, BC(int, x), C, 0xf, 0xf, true)); }
DI float add16(float x) { auto r = __builtin_amdgcn_permlane16_swap(BC(u32, x), BC(u32, x), false, false); return BC(float, r[0]) + BC(float, r[1]); }
DI float add32(float x) { auto r = __builtin_amdgcn_permlane32_swap(BC(u32, x), BC(u32, x), false, false); return BC(float, r[0]) + BC(float, r[1]); }
DI v2u trrd(LDSP char* p) { return BC(v2u, __builtin_amdgcn_ds_read_tr16_b64_v4i16((LDSP s4*)p)); }
DI v4u trfrag(LDSP char* p, int off2) { v2u a = trrd(p), b = trrd(p + off2); return v4u{a.x, a.y, b.x, b.y}; }
DI void bar() { asm volatile("s_waitcnt lgkmcnt(0)\n\ts_barrier" ::: "memory"); }
#define WAITVM(n) asm volatile("s_waitcnt vmcnt(" #n ")" ::: "memory")
#define LD16S(d, voff, base, imm) asm volatile("global_load_dwordx4 %0, %1, %2 offset:%3 sc1" : "=v"(d) : "v"(voff), "s"(base), "i"(imm) : "memory")
#define RLD16S(d, voff, base, imm) asm volatile("global_load_dwordx4 %0, %1, %2 offset:%3 sc1 ; RETRY" : "+v"(d) : "v"(voff), "s"(base), "i"(imm) : "memory")
#define ST16S(d, voff, base, imm) asm volatile("global_store_dwordx4 %1, %0, %2 offset:%3 sc1\n\ts_nop 1" ::"v"(d), "v"(voff), "s"(base), "i"(imm) : "memory")
#define LD16(d, voff, base, imm) asm volatile("global_load_dwordx4 %0, %1, %2 offset:%3" : "=v"(d) : "v"(voff), "s"(base), "i"(imm) : "memory")
#define LD8(d, voff, base, imm) asm volatile("global_load_dwordx2 %0, %1, %2 offset:%3" : "=v"(d) : "v"(voff), "s"(base), "i"(imm) : "memory")
#define PIN(x) asm volatile("" : "+v"(x))
DI void glds(u32 voff, const char* base, u32 dst) {
  asm volatile("s_mov_b32 m0, %2\n\ts_nop 0\n\tglobal_load_lds_dwordx4 %0, %1" ::"v"(voff), "s"(base), "s"(dst) : "memory");
}
DI u32 max3u(u32 a, u32 b, u32 c) { u32 m = a > b ? a : b; return m > c ? m : c; }
DI u32 clampS(u32 a) { return a < 0xfffffffeu ? a : 0xfffffffeu; }
DI void lnscal(float s1, float s2, float& mr, float& r) {
  float m = s1 * (1.f / 512.f), var = fmaxf(s2 * (1.f / 512.f) - m * m, 0.f);
  float sd = rbf(__builtin_amdgcn_sqrtf(rbf(rbf(var) + 1e-5f)));
  r = __builtin_amdgcn_rcpf(sd); mr = rbf(m) * r;
}
DI float geglu(float g, float u) {
  float e = __builtin_amdgcn_exp2f(g * (g * g * -0.10294324f - 2.30220819f));
  return g * u * __builtin_amdgcn_rcpf(1.f + e);
}

#define STAMP(i)
#define D1 0
#define D2 0
#define D3 8

__global__ void __launch_bounds__(256, 1)
fused(const char* __restrict__ x, const char* __restrict__ wr, const char* __restrict__ wi, const char* __restrict__ wg,
      const char* __restrict__ wu, const char* __restrict__ wd, char* __restrict__ out, char* __restrict__ ws) {
  __shared__ __attribute__((aligned(1024))) char smem[163840];
  LDSP char* lds = (LDSP char*)smem;
  STAMP(0);
  const int t = threadIdx.x, l = t & 63, n = l & 15, g = l >> 4;
  const int wv = __builtin_amdgcn_readfirstlane(t >> 6);
  const int w = blockIdx.x, pw = (w & 7) * 32 + (w >> 3);
  const int s2 = pw;
  const int k1c = w & 7, jc = w >> 3;
  const int mb = pw >> 4, nb = pw & 15;
  const int mt5 = pw >> 3, nt5 = pw & 7;
  const u32 MASK = 0x00030003u;

  v4u xa, xb; u32 Ev;
  { u32 o = (s2 + 256 * (t >> 5)) * 1024 + (t & 31) * 16; LD16(xa, o, x, 0); LD16(xb, o, x, 512); }
  { u32 z = 0; asm volatile("global_load_dword %0, %1, %2 sc1" : "=v"(Ev) : "v"(z), "s"(ws) : "memory"); }
  v2u wra, wrb, wia, wib;
  const int kA = 4 * ((jc >> 3) & 1) + 8 * (16 * (jc >> 4) + 2 * (jc & 7));
  { u32 o = ((k1c + 8 * t) * 257 + kA) * 2; LD8(wra, o, wr, 0); LD8(wrb, o, wr, 16); LD8(wia, o, wi, 0); LD8(wib, o, wi, 16); }
  v2u xr[4];
  { u32 o = (s2 + 256 * (n >> 3)) * 1024 + (64 * (n & 7) + 16 * wv + 4 * g) * 2;
#pragma unroll
    for (int i = 0; i < 4; ++i) LD8(xr[i], o, x + i * 524288, 0); }
  v4u wt;
  { int P = t < 192 ? t : 191, sl = P / 6, pc = P % 6;
    int k = 32 * mb + 8 * ((sl >> 2) & 3) + 4 * (sl >> 4) + (sl & 3);
    int col = 1024 + 24 * nb + (pc < 4 ? (pc & 1) * 8 : 16); col = col < 1368 ? col : 1368;
    const char* p = ((pc == 2 || pc == 3 || pc == 5) ? wu : wg) + (k * 1376 + col) * 2;
    asm volatile("global_load_dwordx4 %0, %1, off" : "=v"(wt) : "v"(p) : "memory"); }
  { v4u ff = {~0u, ~0u, ~0u, ~0u}; u32 o = s2 * 8192 + 4096 + t * 16;
    ST16S(ff, o, ws + OFF_HN, -4096); ST16S(ff, o, ws + OFF_HN, 0); ST16S(ff, o, ws + OFF_H, -4096); ST16S(ff, o, ws + OFF_H, 0);
    u32 oa = ((mb * 4 + wv) * 6 * 16 + nb) * 1024 + l * 16;
#pragma unroll
    for (int i = 0; i < 6; ++i) ST16S(ff, oa, ws + OFF_ACT + i * 16384, 0); }
  const u32 wvo = (8 * g + ((l >> 2) & 3)) * 2752 + ((((l >> 1) ^ g) & 1) * 16 + 8 * (l & 1)) * 2;
  const char* wsrc = ((wv & 2) ? wu : wg) + (wv & 1) * 4 * 2752 + nb * 128;
  auto wload = [&](int ks) { glds(wvo, wsrc + ks * 32 * 2752, R0 + ks * 4096 + wv * 1024); glds(wvo, wsrc + ks * 32 * 2752 + 64, R1 + ks * 4096 + wv * 1024); };
#pragma unroll
  for (int ks = 0; ks < 6; ++ks) wload(ks);

  v4u FAr[2], FAi[2], FBr[4][2], FBi[4][2], FX; f4 twc[4], tws[4];
  { const int k1 = 2 * wv + (n >> 3), kd1 = n & 7; const float ph = (float)(s2 * k1) * (1.f / 2048.f);
#pragma unroll
    for (int ks = 0; ks < 2; ++ks) { float c[8], s[8];
#pragma unroll
      for (int j = 0; j < 8; ++j) { int s1 = 4 * ks + 2 * (j >> 2) + (g >> 1), d1 = 4 * (g & 1) + (j & 3);
        float a = (float)((s1 * k1 + d1 * kd1) & 7) * 0.125f + ph; c[j] = cosr(a) * 0.125f; s[j] = sinr(a) * -0.125f; }
      FAr[ks] = v4u{pkh(c[0], c[1]), pkh(c[2], c[3]), pkh(c[4], c[5]), pkh(c[6], c[7])};
      FAi[ks] = v4u{pkh(s[0], s[1]), pkh(s[2], s[3]), pkh(s[4], s[5]), pkh(s[6], s[7])}; }
#pragma unroll
    for (int mt = 0; mt < 4; ++mt) { u32 P[4], Q[4];
#pragma unroll
      for (int r = 0; r < 4; ++r) { int d2 = 16 * mt + 4 * g + r;
        float a = (float)(d2 * kd1) * (1.f / 512.f); twc[mt][r] = cosr(a); tws[mt][r] = sinr(a);
        float b = (float)((d2 * n) & 63) * (1.f / 64.f), c = cosr(b) * 0.125f, s = sinr(b) * 0.125f; P[r] = pkh(c, s); Q[r] = pkh(-s, c); }
      const u32 N = 0x80008000u;
      FBr[mt][0] = v4u{P[0], P[1], P[2], P[3]}; FBi[mt][0] = v4u{Q[0], Q[1], Q[2], Q[3]};
      FBr[mt][1] = v4u{P[0], Q[1], P[2] ^ N, Q[3] ^ N}; FBi[mt][1] = v4u{Q[0], P[1] ^ N, Q[2] ^ N, P[3]}; }
    u32 e0 = n == 0 ? 0xB0000000u : (n == 1 ? 0x00003000u : 0u), e1 = n == 0 ? 0x30000000u : (n == 1 ? 0x0000B000u : 0u);
    FX = v4u{e0, e1, e0, e1}; }
  STAMP(1);
  asm volatile("s_waitcnt vmcnt(32)" : "+v"(xa), "+v"(xb)::"memory");
  STAMP(2);
  { float a1 = 0, a2 = 0, b1 = 0, b2_ = 0; const u32 one = 0x3f803f80u;
#pragma unroll
    for (int i = 0; i < 4; ++i) { a1 = dot2(xa[i], one, a1); a2 = dot2(xa[i], xa[i], a2); b1 = dot2(xb[i], one, b1); b2_ = dot2(xb[i], xb[i], b2_); }
    a1 += b1; a2 += b2_;
    a1 = dppadd<0xB1>(a1); a2 = dppadd<0xB1>(a2); a1 = dppadd<0x4E>(a1); a2 = dppadd<0x4E>(a2);
    a1 = dppadd<0x141>(a1); a2 = dppadd<0x141>(a2); a1 = dppadd<0x140>(a1); a2 = dppadd<0x140>(a2);
    a1 = add16(a1); a2 = add16(a2);
    float mr, r; lnscal(a1, a2, mr, r);
    v4u na, nb_;
#pragma unroll
    for (int i = 0; i < 4; ++i) { na[i] = pkh(blo(xa[i]) * r - mr, bhi(xa[i]) * r - mr); nb_[i] = pkh(blo(xb[i]) * r - mr, bhi(xb[i]) * r - mr); }
    LDSP char* p = lds + BUFA + ((t >> 5) * 8 + ((t >> 3) & 3)) * 160 + (t & 7) * 16;
    *(LDSP v4u*)p = na; *(LDSP v4u*)(p + 640) = nb_; }
  bar();
  u32 tag[4];
  { LDSP char* p = lds + BUFA + (l >> 2) * 160 + (l & 3) * 8;
    v4u Dp[4];
#pragma unroll
    for (int mt = 0; mt < 4; ++mt) { f4 re = {0, 0, 0, 0}, im = {0, 0, 0, 0};
#pragma unroll
      for (int ks = 0; ks < 2; ++ks) { v4u a = trfrag(p + ks * 5120 + mt * 32, 2560); re = mf16(a, FAr[ks], re); im = mf16(a, FAi[ks], im); }
      f4 r2 = re * twc[mt] + im * tws[mt], i2 = im * twc[mt] - re * tws[mt];
      Dp[mt] = v4u{pkh(r2[0], i2[0]), pkh(r2[1], i2[1]), pkh(r2[2], i2[2]), pkh(r2[3], i2[3])}; }
    f4 Or[2] = {{0, 0, 0, 0}, {0, 0, 0, 0}}, Oi[2] = {{0, 0, 0, 0}, {0, 0, 0, 0}}, Ex = {0, 0, 0, 0};
#pragma unroll
    for (int mt = 0; mt < 4; ++mt) { Ex = mf16(Dp[mt], FX, Ex);
#pragma unroll
      for (int nt = 0; nt < 2; ++nt) { Or[nt] = mf16(Dp[mt], FBr[mt][nt], Or[nt]); Oi[nt] = mf16(Dp[mt], FBi[mt][nt], Oi[nt]); } }
    float e1 = BC(float, __builtin_amdgcn_update_dpp(0, BC(int, Ex[0]), 0xB1, 0xf, 0xf, true));
    bool sp = (l & 31) == 0; Or[0][0] += sp ? Ex[0] : 0.f; Oi[0][0] += sp ? e1 : 0.f;
    asm volatile("s_waitcnt vmcnt(31)" : "+v"(Ev)::"memory");
    const u32 E = __builtin_amdgcn_readfirstlane(Ev);
#pragma unroll
    for (int r = 0; r < 4; ++r) { u32 nib = (E + r) & 15; tag[r] = (nib & 3) | ((nib >> 2) << 16); }
    u32 o = (wv * 8 + g) * 65536 + s2 * 256 + n * 16;
#pragma unroll
    for (int nt = 0; nt < 2; ++nt) { v4u v;
#pragma unroll
      for (int r = 0; r < 4; ++r) v[r] = ((pkh(Or[nt][r], Oi[nt][r]) + 0x00020002u) & ~MASK) | tag[r];
      ST16S(v, o, ws + OFF_U + nt * 262144, 0); } }
  const u32 E = __builtin_amdgcn_readfirstlane(Ev);
  STAMP(3);
  asm volatile("s_waitcnt vmcnt(24)" : "+v"(wra), "+v"(wrb), "+v"(wia), "+v"(wib), "+v"(xr[0]), "+v"(xr[1]), "+v"(xr[2]), "+v"(xr[3]), "+v"(wt)::"memory");
  if (wv < 3) { u32 o = (nb * 16 + mb) * 3072 + t * 16; ST16S(wt, o, ws + OFF_W2, 0); }
  { LDSP char* p = lds + BUFC + t * 8;
    *(LDSP v2u*)p = v2u{wra.x, wia.x}; *(LDSP v2u*)(p + 2048) = v2u{wra.y, wia.y}; *(LDSP v2u*)(p + 4096) = v2u{wrb.x, wib.x}; *(LDSP v2u*)(p + 6144) = v2u{wrb.y, wib.y}; }
  if (jc == 0) {
    const unsigned short* R = (const unsigned short*)wr; const unsigned short* I = (const unsigned short*)wi;
    int sp = k1c + 8 * t, sm = (2048 - sp) & 2047;
    float r0 = blo(wra.x), i0 = blo(wia.x), r0m = blo(R[sm * 257]), i0m = blo(I[sm * 257]);
    float r9 = blo(R[sp * 257 + 256]), i9 = blo(I[sp * 257 + 256]), r9m = blo(R[sm * 257 + 256]), i9m = blo(I[sm * 257 + 256]);
    *(LDSP f4*)(lds + BUFC + 8192 + t * 16) = f4{0.5f * (r0 + r0m), 0.5f * (i0 - i0m), 0.5f * (i9 - i9m), -0.5f * (r9 + r9m)};
  }
#pragma unroll
  for (int ks = 6; ks < 11; ++ks) wload(ks);
  bar();
  f4 Wr[2], Wi[2], W9r, W9i, tc2, ts2; v4u Pf, Qf, Pi, Qi;
#pragma unroll
  for (int r = 0; r < 4; ++r) { int m = 4 * g + r;
    v2u v = *(LDSP v2u*)(lds + BUFC + wv * 2048 + (n + 16 * m) * 8);
    Wr[0][r] = 2.f * blo(v.x); Wr[1][r] = 2.f * bhi(v.x); Wi[0][r] = 2.f * blo(v.y); Wi[1][r] = 2.f * bhi(v.y);
    float a = (float)((n * m) & 15) * (1.f / 16.f), c = cosr(a) * 0.25f, s = sinr(a) * 0.25f;
    Pf[r] = pkh(c, s); Qf[r] = pkh(-s, c); Pi[r] = pkh(c, -s); Qi[r] = pkh(s, c);
    float b = (float)(n * m) * (1.f / 256.f); tc2[r] = cosr(b); ts2[r] = sinr(b); }
  const bool spec = jc == 0 && wv == 0;
  W9r = Wr[0]; W9i = Wi[0];
  if (spec) {
#pragma unroll
    for (int r = 0; r < 4; ++r) { f4 v = *(LDSP f4*)(lds + BUFC + 8192 + (n + 16 * (4 * g + r)) * 16); Wr[0][r] = v[0]; Wi[0][r] = v[1]; W9r[r] = v[2]; W9i[r] = v[3]; } }
  for (int i = 0; i < D1; ++i) __builtin_amdgcn_s_sleep(1);
  STAMP(4);
  { u32 slot = ((k1c >> 1) * 2 + (jc >> 4)) * 4 + (k1c & 1) * 2 + ((jc >> 3) & 1);
    u32 o = (32 * (t >> 5) + ((t >> 1) & 15)) * 256 + 2048 + (t & 1) * 16;
    const char* base = ws + OFF_U + slot * 65536 + (jc & 7) * 32;
    v4u a, b;
    for (u32 spins = 0;; ++spins) {
      asm volatile("global_load_dwordx4 %0, %2, %3 offset:-2048 sc1\n\tglobal_load_dwordx4 %1, %2, %3 offset:2048 sc1\n\ts_waitcnt vmcnt(0)" : "=&v"(a), "=&v"(b) : "v"(o), "s"(base) : "memory");
#pragma unroll
      for (int r = 0; r < 4; ++r) { a[r] ^= tag[r]; b[r] ^= tag[r]; }
      u32 bad = (a[0] | a[1] | a[2] | a[3] | b[0] | b[1] | b[2] | b[3]) & MASK;
      if (!__any(bad != 0) || spins > SPIN_CAP) break;
    }
    LDSP char* p = lds + BUFB + (t & 1) * 4096 + (t >> 6) * 256 + ((t >> 1) & 15) * 16 + ((t >> 5) & 1) * 8;
#pragma unroll
    for (int c = 0; c < 4; ++c) *(LDSP v2u*)(p + c * 1024) = v2u{a[c], b[c]}; }
  bar();
  STAMP(5);
  { const f4 z = {0, 0, 0, 0};
    auto pk4 = [](f4 a, f4 b) { return v4u{pkh(a[0], b[0]), pkh(a[1], b[1]), pkh(a[2], b[2]), pkh(a[3], b[3])}; };
    auto inv = [&](f4 xr_, f4 xi_, f4 fr, f4 fi) {
      v4u y = pk4(xr_ * fr - xi_ * fi, xr_ * fi + xi_ * fr);
      f4 re = mf16(y, Pi, z), im = mf16(y, Qi, z);
      y = pk4(re * tc2 - im * ts2, im * tc2 + re * ts2);
      return pk4(mf16(Pi, y, z), mf16(Qi, y, z)); };
    f4 Xr[2], Xi[2]; v4u o[2];
#pragma unroll
    for (int c = 0; c < 2; ++c) { v4u a = *(LDSP v4u*)(lds + BUFB + (2 * wv + c) * 1024 + l * 16);
      f4 re = mf16(a, Pf, z), im = mf16(a, Qf, z);
      a = pk4(re * tc2 + im * ts2, im * tc2 - re * ts2);
      Xr[c] = mf16(Pf, a, z); Xi[c] = mf16(Qf, a, z);
      o[c] = inv(Xr[c], Xi[c], Wr[c], Wi[c]); }
    LDSP char* p = lds + BUFA + (wv >> 1) * 4224 + (64 * g + n) * 16 + (wv & 1) * 8;
#pragma unroll
    for (int r = 0; r < 4; ++r) *(LDSP v2u*)(p + r * 256) = v2u{o[0][r], o[1][r]};
    if (spec) { v4u q = inv(Xr[0], Xi[0], W9r, W9i); u32 oo = k1c * 4096 + (64 * g + n) * 16;
      v4u q0 = {q[0], E ^ 0x9e3779b9u, E + 0x7f4a7c15u, ~E}, q1 = q0, q2 = q0, q3 = q0; q1[0] = q[1]; q2[0] = q[2]; q3[0] = q[3];
      ST16S(q0, oo, ws + OFF_V9, 0); ST16S(q1, oo, ws + OFF_V9, 256); ST16S(q2, oo, ws + OFF_V9, 512); ST16S(q3, oo, ws + OFF_V9, 768); } }
  bar();
  { LDSP char* p = lds + BUFA + (t & 1) * 4224 + (t >> 1) * 16;
    v4u a = *(LDSP v4u*)p, b = *(LDSP v4u*)(p + 2048);
#pragma unroll
    for (int r = 0; r < 4; ++r) { a[r] = ((a[r] + 0x00020002u) & ~MASK) | tag[r]; b[r] = ((b[r] + 0x00020002u) & ~MASK) | tag[r]; }
    u32 o = (t >> 4) * 65536 + pw * 256 + (t & 15) * 16;
    ST16S(a, o, ws + OFF_V, 0); ST16S(b, o, ws + OFF_V + 16 * 65536, 0); }
  STAMP(6);
#pragma unroll
  for (int ks = 11; ks < 16; ++ks) wload(ks);
  v4u F3r[2], F3i[2], G[4][4]; f4 t3c[4], t3s[4], Y[4];
  { const int d2 = 16 * wv + n, gh = g >> 1, nh = n >> 3;
#pragma unroll
    for (int ks = 0; ks < 2; ++ks)
#pragma unroll
      for (int p = 0; p < 4; ++p) { int kd2 = 16 * ks + 4 * g + 2 * (p & 1) + (p >> 1);
        float a = (float)((kd2 * d2) & 63) * (1.f / 64.f), c = cosr(a) * 0.125f, s = sinr(a) * 0.125f; F3r[ks][p] = pkh(c, -s); F3i[ks][p] = pkh(s, c); }
#pragma unroll
    for (int mt = 0; mt < 4; ++mt)
#pragma unroll
      for (int r = 0; r < 4; ++r) { float a = (float)(4 * d2 * (4 * (g & 1) + r) + s2 * (2 * mt + gh)) * (1.f / 2048.f); t3c[mt][r] = cosr(a); t3s[mt][r] = sinr(a); }
    v4u B0, B1;
#pragma unroll
    for (int r = 0; r < 4; ++r) { float a = (float)((nh * gh + (n & 7) * (4 * (g & 1) + r)) & 7) * 0.125f, c = cosr(a) * 0.125f, s = sinr(a) * 0.125f; B0[r] = pkh(c, -s); B1[r] = pkh(-s, -c); }
#pragma unroll
    for (int mt = 0; mt < 4; ++mt)
#pragma unroll
      for (int nt = 0; nt < 4; ++nt) { int p = 2 * mt * nt + nt * gh + mt * nh; u32 ng = (p & 2) ? 0x80008000u : 0u;
#pragma unroll
        for (int r = 0; r < 4; ++r) G[mt][nt][r] = ((p & 1) ? B1[r] : B0[r]) ^ ng; }
#pragma unroll
    for (int i = 0; i < 4; ++i) Y[i] = f4{blo(xr[i].x), bhi(xr[i].x), blo(xr[i].y), bhi(xr[i].y)}; }
  for (int i = 0; i < D2; ++i) __builtin_amdgcn_s_sleep(1);
  STAMP(7);
  { u32 o = (t >> 1) * 512 + (t & 1) * 16, o9 = (l & 7) * 4096 + s2 * 16;
    const char* base = ws + OFF_V + (s2 >> 3) * 65536 + (s2 & 7) * 32;
    v4u a, b, c9 = {0u, E ^ 0x9e3779b9u, E + 0x7f4a7c15u, ~E};
    for (u32 spins = 0;; ++spins) {
      if (wv == 0) RLD16S(c9, o9, ws + OFF_V9, 0);
      asm volatile("global_load_dwordx4 %0, %3, %4 sc1\n\tglobal_load_dwordx4 %1, %3, %4 offset:256 sc1\n\ts_waitcnt vmcnt(0)" : "=&v"(a), "=&v"(b), "+v"(c9) : "v"(o), "s"(base) : "memory");
#pragma unroll
      for (int r = 0; r < 4; ++r) { a[r] ^= tag[r]; b[r] ^= tag[r]; }
      u32 bad = ((a[0] | a[1] | a[2] | a[3] | b[0] | b[1] | b[2] | b[3]) & MASK) | (c9[1] ^ E ^ 0x9e3779b9u) | (c9[2] ^ (E + 0x7f4a7c15u)) | (c9[3] ^ ~E);
      if (!__any(bad != 0) || spins > SPIN_CAP) break;
    }
    const int u = t >> 1, gp = u & 3;
    u32 ad = BUFB + ((u >> 5) * 2 + ((u >> 3) & 1)) * 1024 + gp * 256 + (((u >> 4) & 1) * 8 + ((u >> 2) & 1) * 4 + gp) * 16 + (t & 1) * 8;
#pragma unroll
    for (int c = 0; c < 4; ++c) *(LDSP v2u*)(lds + (ad ^ (c * 16))) = v2u{a[c], b[c]};
    if (t < 8) *(LDSP u32*)(lds + BUFB + 8192 + (t & 1) * 16 + (t >> 1) * 4) = c9[0]; }
  bar();
  STAMP(8);
  if (w == 0 && t == 0) { u32 z = 0, e1 = E + 1; asm volatile("global_store_dword %0, %1, %2 sc1" ::"v"(z), "v"(e1), "s"(ws) : "memory"); }
  v4u W2p[12];
  { u32 o = nb * 49152 + wv * 12288 + 4096 + l * 16;
#pragma unroll
    for (int i = 0; i < 8; ++i) LD16S(W2p[i], o, ws + OFF_W2, i * 1024 - 4096);
#pragma unroll
    for (int i = 8; i < 12; ++i) LD16S(W2p[i], o, ws + OFF_W2 + 8192, i * 1024 - 12288); }
  { v4u v9 = *(LDSP v4u*)(lds + BUFB + 8192 + (g >> 1) * 16);
    const float sg = (g & 1) ? 0.f : ((n & 1) ? -0.125f : 0.125f);
    LDSP char* p = lds + BUFB + g * 256 + (n ^ g) * 16;
#pragma unroll
    for (int mt = 0; mt < 4; ++mt) { h2 e = BC(h2, v9[mt]); f4 re = {(float)e[0] * sg, 0, 0, 0}, im = {(float)e[1] * sg, 0, 0, 0};
#pragma unroll
      for (int ks = 0; ks < 2; ++ks) { v4u a = *(LDSP v4u*)(p + (mt * 2 + ks) * 1024); re = mf16(a, F3r[ks], re); im = mf16(a, F3i[ks], im); }
      f4 r2 = re * t3c[mt] - im * t3s[mt], i2 = im * t3c[mt] + re * t3s[mt];
      v4u a = {pkh(r2[0], i2[0]), pkh(r2[1], i2[1]), pkh(r2[2], i2[2]), pkh(r2[3], i2[3])};
#pragma unroll
      for (int nt = 0; nt < 4; ++nt) Y[nt] = mf16(a, G[mt][nt], Y[nt]); }
    u32 ad = BUFA + (8 * (n & 7) + 2 * wv + (g >> 1)) * 128 + ((n >> 3) ^ (2 * (n & 3))) * 16 + (g & 1) * 8;
    float a1[4], a2[4]; const u32 one = 0x3f803f80u;
#pragma unroll
    for (int nt = 0; nt < 4; ++nt) { v2u h = {pkb(Y[nt][0], Y[nt][1]), pkb(Y[nt][2], Y[nt][3])};
      *(LDSP v2u*)(lds + (ad ^ (nt * 32))) = h;
      a1[nt] = dot2(h.y, one, dot2(h.x, one, 0.f)); a2[nt] = dot2(h.y, h.y, dot2(h.x, h.x, 0.f)); }
#pragma unroll
    for (int nt = 0; nt < 4; ++nt) { a1[nt] = dppadd<0x141>(dppadd<0x4E>(dppadd<0xB1>(a1[nt]))); a2[nt] = dppadd<0x141>(dppadd<0x4E>(dppadd<0xB1>(a2[nt]))); }
    if ((n & 7) == 0) { LDSP char* q = lds + BUFA + 8192 + (2 * wv + (g >> 1)) * 128 + (n >> 3) * 16 + (g & 1) * 8;
#pragma unroll
      for (int nt = 0; nt < 4; ++nt) *(LDSP f2*)(q + nt * 32) = f2{a1[nt], a2[nt]}; } }
  bar();
  { f4 st = *(LDSP f4*)(lds + BUFA + 8192 + l * 16);
    LDSP char* p = lds + BUFA + (t >> 3) * 128 + ((t & 7) ^ (2 * wv)) * 16;
    v4u ha = *(LDSP v4u*)p, hb = *(LDSP v4u*)(p + 4096);
    float a1 = st[0] + st[2], a2 = st[1] + st[3];
    a1 = add32(add16(dppadd<0x128>(a1))); a2 = add32(add16(dppadd<0x128>(a2)));
    float mr, r; lnscal(a1, a2, mr, r);
    v4u na, nb_;
#pragma unroll
    for (int i = 0; i < 4; ++i) { na[i] = pkb(blo(ha[i]) * r - mr, bhi(ha[i]) * r - mr); nb_[i] = pkb(blo(hb[i]) * r - mr, bhi(hb[i]) * r - mr); }
    na[0] = clampS(na[0]); na[2] = clampS(na[2]); nb_[0] = clampS(nb_[0]); nb_[2] = clampS(nb_[2]);
    u32 o = s2 * 8192 + 4096 + t * 16;
    ST16S(na, o, ws + OFF_HN, -4096); ST16S(nb_, o, ws + OFF_HN, 0);
    ha[0] = clampS(ha[0]); ha[2] = clampS(ha[2]); hb[0] = clampS(hb[0]); hb[2] = clampS(hb[2]);
    ST16S(ha, o, ws + OFF_H, -4096); ST16S(hb, o, ws + OFF_H, 0); }
  STAMP(9);
  for (int i = 0; i < D3; ++i) __builtin_amdgcn_s_sleep(1);

  v4u A[2][16];
  const u32 ao = (16 * mb + 4 * wv + (n >> 3)) * 8192 + 4096 + g * 128 + (n & 7) * 16;
  const char* ab0 = ws + OFF_HN; const char* ab1 = ws + OFF_HN + 16384;
#define ALOAD(c) _Pragma("unroll") for (int i = 0; i < 4; ++i) { LD16S(A[0][4 * (c) + i], ao, ab0, (4 * (c) + i) * 512 - 4096); LD16S(A[1][4 * (c) + i], ao, ab1, (4 * (c) + i) * 512 - 4096); }
#define AWAIT(c, N) AWAITX(c, N, "")
#define AWAITX(c, N, X) asm volatile("s_waitcnt vmcnt(" #N ")" X : "+v"(A[0][4 * (c)]), "+v"(A[0][4 * (c) + 1]), "+v"(A[0][4 * (c) + 2]), "+v"(A[0][4 * (c) + 3]), \
                                 "+v"(A[1][4 * (c)]), "+v"(A[1][4 * (c) + 1]), "+v"(A[1][4 * (c) + 2]), "+v"(A[1][4 * (c) + 3])::"memory")
#define CNT(i)
#define ACHECK(c) for (u32 spins = 0; spins < SPIN_CAP; ++spins) { u32 m = 0; \
    _Pragma("unroll") for (int i = 0; i < 4; ++i) { m = max3u(m, A[0][4 * (c) + i][0], A[0][4 * (c) + i][2]); m = max3u(m, A[1][4 * (c) + i][0], A[1][4 * (c) + i][2]); } \
    if (!__any(m == ~0u)) break; CNT(2); \
    _Pragma("unroll") for (int i = 0; i < 4; ++i) { RLD16S(A[0][4 * (c) + i], ao, ab0, (4 * (c) + i) * 512 - 4096); RLD16S(A[1][4 * (c) + i], ao, ab1, (4 * (c) + i) * 512 - 4096); } \
    AWAITX(c, 0, " ; RETRY"); }
  ALOAD(0) ALOAD(1)
  LDSP char* wp0 = lds + (l >> 2) * 64 + (g & 1) * 32 + (l & 3) * 8;
  LDSP char* wp1 = lds + (l >> 2) * 64 + ((g & 1) ^ 1) * 32 + (l & 3) * 8;
  f4 acc0[2][4], acc1[2][4], acc2[2][3];
  const f4 z4 = {0, 0, 0, 0};
#pragma unroll
  for (int i = 0; i < 4; ++i) { acc0[0][i] = z4; acc0[1][i] = z4; acc1[0][i] = z4; acc1[1][i] = z4; }
#pragma unroll
  for (int i = 0; i < 3; ++i) { acc2[0][i] = z4; acc2[1][i] = z4; }
  auto kstep = [&](f4 (&acc)[2][4], u32 R, int ks) {
    v4u b0 = trfrag(wp0 + R + ks * 4096, 1024), b1 = trfrag(wp0 + R + ks * 4096 + 2048, 1024);
    v4u b2_ = trfrag(wp1 + R + ks * 4096, 1024), b3 = trfrag(wp1 + R + ks * 4096 + 2048, 1024);
#pragma unroll
    for (int rt = 0; rt < 2; ++rt) { acc[rt][0] = mb16(b0, A[rt][ks], acc[rt][0]); acc[rt][1] = mb16(b1, A[rt][ks], acc[rt][1]);
      acc[rt][2] = mb16(b2_, A[rt][ks], acc[rt][2]); acc[rt][3] = mb16(b3, A[rt][ks], acc[rt][3]); } };
  auto actfrag = [&](f4 (&a)[4]) { v4u v = {pkb(geglu(a[0][0], a[1][0]), geglu(a[0][1], a[1][1])), pkb(geglu(a[0][2], a[1][2]), geglu(a[0][3], a[1][3])),
                                            pkb(geglu(a[2][0], a[3][0]), geglu(a[2][1], a[3][1])), pkb(geglu(a[2][2], a[3][2]), geglu(a[2][3], a[3][3]))};
    v[0] = clampS(v[0]); v[2] = clampS(v[2]); return v; };
  const u32 so = ((mb * 4 + wv) * 6 * 16 + nb) * 1024 + l * 16;
  const u32 dvo = (l >> 3) * 1024 + ((((l >> 1) ^ (l >> 4)) & 3) * 16 + (l & 1) * 8) * 2;
  const char* dsrc = wd + nt5 * 128;
  auto dload = [&](int b, int i, u32 dst) { const char* s = dsrc + (64 * i + 32 * b + 8 * wv) * 1024; glds(dvo, s, dst + wv * 1024); glds(dvo, s + 512 * 1024, dst + 4096 + wv * 1024); };
  constexpr u32 PD0[8] = {R2, R2 + 8192, R2 + 16384, R2 + 24576, R0 + 49152, R0 + 57344, R1, R1 + 8192};
  constexpr u32 PD1[8] = {R1 + 16384, R1 + 24576, R1 + 32768, R1 + 40960, R1 + 49152, R1 + 57344, R0, R0 + 8192};

  AWAIT(0, 8);
  STAMP(10);
  ACHECK(0)
  ALOAD(2) ALOAD(3)
#pragma unroll
  for (int ks = 0; ks < 4; ++ks) kstep(acc0, R0, ks);
  AWAIT(1, 16); ACHECK(1)
#pragma unroll
  for (int ks = 4; ks < 8; ++ks) kstep(acc0, R0, ks);
  AWAIT(2, 8); ACHECK(2)
#pragma unroll
  for (int ks = 8; ks < 12; ++ks) kstep(acc0, R0, ks);
  AWAIT(3, 0); ACHECK(3)
  asm volatile("" : "+v"(W2p[0]), "+v"(W2p[1]), "+v"(W2p[2]), "+v"(W2p[3]), "+v"(W2p[4]), "+v"(W2p[5]), "+v"(W2p[6]), "+v"(W2p[7]), "+v"(W2p[8]), "+v"(W2p[9]), "+v"(W2p[10]), "+v"(W2p[11]));
#pragma unroll
  for (int ks = 12; ks < 16; ++ks) kstep(acc0, R0, ks);
  bar();
  STAMP(11);
#pragma unroll
  for (int i = 0; i < 12; ++i) *(LDSP v4u*)(lds + R0 + wv * 12288 + i * 1024 + l * 16) = W2p[i];
#pragma unroll
  for (int ks = 0; ks < 16; ++ks) {
    if (ks < 6) dload(0, ks, PD0[ks]);
    kstep(acc1, R1, ks);
    if (ks == 7) { v4u v = actfrag(acc0[0]); ST16S(v, so, ws + OFF_ACT, 0); }
    if (ks == 15) { v4u v = actfrag(acc0[1]); ST16S(v, so, ws + OFF_ACT + 3 * 16384, 0); } }
  bar();
  STAMP(12);
  { LDSP char* p = lds + R0 + (l >> 2) * 96 + (l & 3) * 8;
#pragma unroll
    for (int ks = 0; ks < 16; ++ks) {
      if (ks < 2) dload(0, 6 + ks, PD0[6 + ks]); else if (ks < 8) dload(1, ks - 2, PD1[ks - 2]);
      v4u b0 = trfrag(p + ks * 3072, 1536), b1 = trfrag(p + ks * 3072 + 32, 1536), b2_ = trfrag(p + ks * 3072 + 64, 1536);
#pragma unroll
      for (int rt = 0; rt < 2; ++rt) { acc2[rt][0] = mb16(b0, A[rt][ks], acc2[rt][0]); acc2[rt][1] = mb16(b1, A[rt][ks], acc2[rt][1]); acc2[rt][2] = mb16(b2_, A[rt][ks], acc2[rt][2]); }
      if (ks == 7) { v4u v = actfrag(acc1[0]); ST16S(v, so, ws + OFF_ACT + 16384, 0); }
      if (ks == 15) { v4u v = actfrag(acc1[1]); ST16S(v, so, ws + OFF_ACT + 4 * 16384, 0); } } }
  bar();
  STAMP(13);
  const int mh = wv >> 1, kh = wv & 1;
  const u32 po = (((mt5 >> 1) * 4 + (mt5 & 1) * 2 + mh) * 6 * 16 + 8 * kh) * 1024 + 4096 + l * 16;
  const char* pb0 = ws + OFF_ACT + kh * 3 * 16384; const char* pb1 = ws + OFF_ACT + (kh ^ 1) * 3 * 16384;
  v4u P0[2][8], P1[2][8], P2[2][8], hh[2];
#define PLOAD(P, b) _Pragma("unroll") for (int i = 0; i < 8; ++i) { LD16S(P[0][i], po, pb0 + (b) * 16384, i * 1024 - 4096); LD16S(P[1][i], po, pb1 + (b) * 16384, i * 1024 - 4096); }
#define PWAIT(P, c, N) PWAITX(P, c, N, "")
#define PWAITX(P, c, N, X) asm volatile("s_waitcnt vmcnt(" #N ")" X : "+v"(P[0][4 * (c)]), "+v"(P[0][4 * (c) + 1]), "+v"(P[0][4 * (c) + 2]), "+v"(P[0][4 * (c) + 3]), \
                                 "+v"(P[1][4 * (c)]), "+v"(P[1][4 * (c) + 1]), "+v"(P[1][4 * (c) + 2]), "+v"(P[1][4 * (c) + 3])::"memory")
#define PCHECK(P, b, c) for (u32 spins = 0; spins < SPIN_CAP; ++spins) { u32 m = 0; \
    _Pragma("unroll") for (int i = 4 * (c); i < 4 * (c) + 4; ++i) { m = max3u(m, P[0][i][0], P[0][i][2]); m = max3u(m, P[1][i][0], P[1][i][2]); } \
    if (!__any(m == ~0u)) break; CNT(3); \
    _Pragma("unroll") for (int i = 4 * (c); i < 4 * (c) + 4; ++i) { RLD16S(P[0][i], po, pb0 + (b) * 16384, i * 1024 - 4096); RLD16S(P[1][i], po, pb1 + (b) * 16384, i * 1024 - 4096); } \
    PWAITX(P, c, 0, " ; RETRY"); }
  const int prod5 = 8 * mt5 + 4 * mh + 2 * kh + (n >> 3);
  const u32 ho = prod5 * 8192 + (8 * nt5 + 2 * (g & 1) + (g >> 1)) * 128 + (n & 7) * 16;
  LD16S(hh[0], ho, ws + OFF_H, 0); LD16S(hh[1], ho, ws + OFF_H, 512);
  PLOAD(P0, 0)
#pragma unroll
  for (int rt = 0; rt < 2; ++rt) { f4 m = acc2[rt][2];
    auto r0 = __builtin_amdgcn_permlane32_swap(BC(u32, m[0]), BC(u32, m[2]), false, false);
    auto r1 = __builtin_amdgcn_permlane32_swap(BC(u32, m[1]), BC(u32, m[3]), false, false);
    v4u v = {pkb(geglu(acc2[rt][0][0], acc2[rt][1][0]), geglu(acc2[rt][0][1], acc2[rt][1][1])), pkb(geglu(acc2[rt][0][2], acc2[rt][1][2]), geglu(acc2[rt][0][3], acc2[rt][1][3])),
             pkb(geglu(BC(float, r0[0]), BC(float, r0[1])), geglu(BC(float, r1[0]), BC(float, r1[1]))), 0u};
    v[0] = nb < 15 ? clampS(v[0]) : 0u; v[1] = nb < 15 ? v[1] : 0u; v[2] = nb < 14 ? clampS(v[2]) : 0u;
    ST16S(v, so, ws + OFF_ACT + (3 * rt + 2) * 16384, 0); }
  STAMP(14);
  dload(1, 6, PD1[6]); dload(1, 7, PD1[7]);
  auto tload = [&](int pc, u32 dst) { int r = 1024 + 8 * pc; r = r < 1368 ? r : 1368; glds(dvo, dsrc + r * 1024, dst); };
#pragma unroll
  for (int i = 0; i < 8; ++i) { int j = 4 * i + wv; j = j < 30 ? j : 29; int hf = j >= 15, pc = j - 15 * hf; tload(pc + 24 * hf, R0 + 16384 + j * 1024); }
  PLOAD(P1, 1)
  f4 acc[2][4];
  asm volatile("s_waitcnt vmcnt(46)" : "+v"(hh[0]), "+v"(hh[1])::"memory");
  for (u32 spins = 0; spins < SPIN_CAP; ++spins) {
    if (!__any(max3u(max3u(hh[0][0], hh[0][2], hh[1][0]), hh[1][2], 0u) == ~0u)) break;
    RLD16S(hh[0], ho, ws + OFF_H, 0); RLD16S(hh[1], ho, ws + OFF_H, 512);
    asm volatile("s_waitcnt vmcnt(0) ; RETRY" : "+v"(hh[0]), "+v"(hh[1])::"memory"); }
#pragma unroll
  for (int u = 0; u < 2; ++u) {
    auto r0 = __builtin_amdgcn_permlane16_swap(hh[u][0], hh[u][2], false, false);
    auto r1 = __builtin_amdgcn_permlane16_swap(hh[u][1], hh[u][3], false, false);
    acc[0][2 * u] = f4{blo(r0[0]), bhi(r0[0]), blo(r1[0]), bhi(r1[0])}; acc[0][2 * u + 1] = f4{blo(r0[1]), bhi(r0[1]), blo(r1[1]), bhi(r1[1])};
    acc[1][2 * u] = z4; acc[1][2 * u + 1] = z4; }
  bar();
  u32 dp[4];
#pragma unroll
  for (int c = 0; c < 4; ++c) dp[c] = (l >> 2) * 128 + ((c ^ (l >> 3)) & 3) * 32 + (l & 3) * 8;
  auto dstep = [&](v4u a0, v4u a1, u32 base) {
#pragma unroll
    for (int c = 0; c < 4; ++c) { v4u b = trfrag(lds + base + kh * 4096 + dp[c], 2048); acc[0][c] = mb16(b, a0, acc[0][c]); acc[1][c] = mb16(b, a1, acc[1][c]); } };
  PWAIT(P0, 0, 38); PCHECK(P0, 0, 0)
#pragma unroll
  for (int i = 0; i < 4; ++i) dstep(P0[0][i], P0[1][i], PD0[i]);
  PWAIT(P0, 1, 30); PCHECK(P0, 0, 1)
#pragma unroll
  for (int i = 4; i < 8; ++i) dstep(P0[0][i], P0[1][i], PD0[i]);
  WAITVM(16);
  bar();
  STAMP(15);
#pragma unroll
  for (int i = 0; i < 5; ++i) { int j = 4 * i + wv; j = j < 18 ? j : 17; int hf = j >= 9, pc = j - 9 * hf; tload(15 + pc + 24 * hf, R2 + j * 1024); }
  PLOAD(P2, 2)
  PWAIT(P1, 0, 29); PCHECK(P1, 1, 0)
#pragma unroll
  for (int i = 0; i < 4; ++i) dstep(P1[0][i], P1[1][i], PD1[i]);
  PWAIT(P1, 1, 21); PCHECK(P1, 1, 1)
#pragma unroll
  for (int i = 4; i < 8; ++i) dstep(P1[0][i], P1[1][i], PD1[i]);
  WAITVM(16);
  bar();
  STAMP(16);
  { const int rho = 4 * (g & 1) + 2 * (g >> 1) + ((l >> 2) & 1);
    u32 dq[4];
#pragma unroll
    for (int c = 0; c < 4; ++c) dq[c] = 2048 + rho * 128 + ((c ^ (rho >> 1)) & 3) * 32 + (l & 3) * 8;
    auto tstep = [&](int i) { u32 base = i < 5 ? R0 + 16384 + kh * 15360 + i * 3072 : R2 + kh * 9216 + (i - 5) * 3072;
#pragma unroll
      for (int c = 0; c < 4; ++c) { v2u x0 = trrd(lds + base + dp[c]), x1 = trrd(lds + base + dq[c]); v4u b = {x0.x, x0.y, x1.x, x1.y};
        acc[0][c] = mb16(b, P2[0][i], acc[0][c]); acc[1][c] = mb16(b, P2[1][i], acc[1][c]); } };
    PWAIT(P2, 0, 8); PCHECK(P2, 2, 0)
#pragma unroll
    for (int i = 0; i < 4; ++i) tstep(i);
    PWAIT(P2, 1, 0); PCHECK(P2, 2, 1)
#pragma unroll
    for (int i = 4; i < 8; ++i) tstep(i); }
  STAMP(17);
#pragma unroll
  for (int c = 0; c < 4; ++c) *(LDSP f4*)(lds + R1 + wv * 4096 + c * 1024 + l * 16) = acc[1][c];
  bar();
  { u32 oo = (prod5 + 256 * (n & 7)) * 1024 + nt5 * 128 + (2 * (g & 1) + (g >> 1)) * 16;
#pragma unroll
    for (int u = 0; u < 2; ++u) {
      f4 a = acc[0][2 * u] + *(LDSP f4*)(lds + R1 + (wv ^ 1) * 4096 + (2 * u) * 1024 + l * 16);
      f4 b = acc[0][2 * u + 1] + *(LDSP f4*)(lds + R1 + (wv ^ 1) * 4096 + (2 * u + 1) * 1024 + l * 16);
      auto r0 = __builtin_amdgcn_permlane16_swap(pkb(a[0], a[1]), pkb(b[0], b[1]), false, false);
      auto r1 = __builtin_amdgcn_permlane16_swap(pkb(a[2], a[3]), pkb(b[2], b[3]), false, false);
      *(v4u*)(out + oo + u * 64) = v4u{r0[0], r1[0], r0[1], r1[1]}; } }
}

extern "C" void kernel_launch(void* const* d_in, const int* in_sizes, int n_in, void* d_out, int out_size, void* d_ws, size_t ws_size, hipStream_t stream) {
  fused<<<256, 256, 0, stream>>>((const char*)d_in[0], (const char*)d_in[1], (const char*)d_in[2], (const char*)d_in[4], (const char*)d_in[5],
                                 (const char*)d_in[6], (char*)d_out, (char*)d_ws);
}
```

```cpp
#include <hip/hip_runtime.h>

__global__ void __launch_bounds__(256, 1) __attribute__((aligned(4096)))
fused(const void* x, const void* w_r, const void* w_i, const void* w_gate, const void* w_up, const void* w_down, void* out, void* ws) {
  __shared__ __attribute__((aligned(1024))) char smem[163840];
  auto ka = __builtin_amdgcn_kernarg_segment_ptr();
  asm volatile(
    ".Lbeg_%=:\n"
    "s_load_dwordx16 s[4:19], s[0:1], 0\n"
    "s_getpc_b64 s[20:21]\n"
    "s_and_b32 s22, s2, 7\n"
    "s_lshr_b32 s23, s2, 3\n"
    "s_lshr_b32 s28, s22, 1\n"
    "s_lshr_b32 s29, s23, 3\n"
    "s_lshl2_add_u32 s24, s28, s29\n"
    "s_and_b32 s28, s22, 1\n"
    "s_and_b32 s29, s23, 7\n"
    "s_lshl3_add_u32 s25, s28, s29\n"
    "s_lshl4_add_u32 s26, s24, s25\n"
    "v_lshrrev_b32 v3, 5, v0\n"
    "v_lshlrev_b32 v2, 18, v3\n"
    "v_lshlrev_b32 v4, 4, v0\n"
    "v_and_b32 v3, 0x1f0, v4\n"
    "s_lshl_b32 s28, s26, 10\n"
    "v_or3_b32 v2, v2, v3, s28\n"
    "v_lshrrev_b32 v3, 6, v0\n"
    "v_and_b32 v14, 15, v0\n"
    "v_readfirstlane_b32 s27, v3\n"
    "v_and_b32 v5, 63, v0\n"
    "v_bfe_u32 v15, v0, 4, 2\n"
    "v_and_b32 v16, 0x3f0, v4\n"
    "s_and_b32 s28, s23, 7\n"
    "s_lshl2_add_u32 s28, s28, s27\n"
    "s_lshl_b32 s28, s28, 10\n"
    "v_and_b32 v3, 7, v0\n"
    "v_lshl_add_u32 v3, v3, 7, s28\n"
    "s_mov_b32 s29, .Lend_%=-.Lbeg_%=-64\n"
    "v_min_u32 v3, s29, v3\n"
    "global_load_dword v17, v3, s[20:21]\n"
    "s_branch .Lpad_%=\n"
    ".fill 17, 4, 0xbf800000\n"
    ".Lpad_%=:\n"
    "s_waitcnt lgkmcnt(0)\n"
    "global_load_dwordx4 v[6:9], v2, s[4:5]  nt\n"
    "global_load_dwordx4 v[10:13], v2, s[4:5] offset:512 nt\n"
    "s_load_dword s34, s[18:19], 0\n"
    "s_lshl_b32 s35, s27, 12\n"
    "s_lshl_b32 s36, s27, 11\n"
    "s_lshl_b32 s37, s27, 10\n"
    "s_bitcmp1_b32 s27, 0\n"
    "s_cselect_b64 s[20:21], s[8:9], s[6:7]\n"
    "s_lshr_b32 s28, s27, 1\n"
    "s_lshl1_add_u32 s28, s23, s28\n"
    "s_lshl_b32 s28, s28, 5\n"
    "s_add_u32 s28, s28, s22\n"
    "v_lshl_add_u32 v3, v15, 3, s28\n"
    "v_mul_u32_u24 v3, 0x202, v3\n"
    "v_lshlrev_b32 v2, 7, v14\n"
    "v_min_u32 v2, 0x1fe, v2\n"
    "v_add_u32_e64 v3, v3, v2\n"
    "global_load_dword v19, v3, s[20:21]\n"
    "v_bfe_u32 v23, v0, 3, 1\n"
    "v_bfe_u32 v24, v0, 5, 1\n"
    "v_and_b32 v26, 7, v0\n"
    "s_lshl_b32 s28, s27, 1\n"
    "v_bfe_u32 v25, v0, 4, 1\n"
    "v_add_u32 v26, 1, v26\n"
    "v_add_u32 v27, s28, v23\n"
    "v_lshl_add_u32 v31, v24, 8, s26\n"
    "v_mul_u32_u24 v31, v31, v27\n"
    "v_mul_u32_u24 v32, v25, v26\n"
    "v_lshl_add_u32 v31, v32, 10, v31\n"
    "v_cvt_f32_u32 v30, v31\n"
    "v_cvt_f32_u32 v29, v27\n"
    "v_mul_f32 v30, 0x3a000000, v30\n"
    "v_cvt_f32_u32_e64 v28, v26\n"
    "v_fmamk_f32 v33, v29, 0x3e800000, v30\n"
    "v_fmamk_f32 v54, v28, 0x3e000000, v30\n"
    "v_fmamk_f32 v57, v28, 0x3e800000, v30\n"
    "v_fmamk_f32 v60, v28, 0x3ec00000, v30\n"
    "v_fmamk_f32 v65, v28, 0x3e000000, v33\n"
    "v_fmamk_f32 v68, v28, 0x3e800000, v33\n"
    "v_fmamk_f32 v71, v28, 0x3ec00000, v33\n"
    "s_and_b32 s30, s23, 8\n"
    "s_and_b32 s31, s23, 16\n"
    "s_lshl_b32 s31, s31, 4\n"
    "s_add_u32 s30, s30, s31\n"
    "s_and_b32 s31, s23, 7\n"
    "s_lshl_b32 s31, s31, 5\n"
    "s_add_u32 s30, s30, s31\n"
    "v_cos_f32 v52, v30\n"
    "v_sin_f32 v53, v30\n"
    "v_cos_f32 v55, v54\n"
    "v_sin_f32 v56, v54\n"
    "v_cos_f32 v58, v57\n"
    "v_sin_f32 v59, v57\n"
    "v_cos_f32 v61, v60\n"
    "v_sin_f32 v62, v60\n"
    "v_cos_f32 v63, v33\n"
    "v_sin_f32 v64, v33\n"
    "v_cos_f32 v66, v65\n"
    "v_sin_f32 v67, v65\n"
    "v_cos_f32 v69, v68\n"
    "v_sin_f32 v70, v68\n"
    "v_cos_f32 v72, v71\n"
    "v_sin_f32 v73, v71\n"
    "s_add_u32 s30, s30, 2\n"
    "v_cvt_f32_u32 v74, v15\n"
    "v_lshrrev_b32 v79, 1, v0\n"
    "s_add_u32 s38, s6, 0x80800\n"
    "v_mul_u32_u24 v50, 0x8000, v23\n"
    "v_and_b32 v51, 1, v0\n"
    "v_mul_f32 v74, 4.0, v74\n"
    "v_cvt_f32_u32 v75, v14\n"
    "v_lshl_add_u32 v79, v79, 3, s22\n"
    "s_addc_u32 s39, s7, 0\n"
    "v_lshl_or_b32 v50, v50, 16, v50\n"
    "v_mul_u32_u24 v51, 0x8000, v51\n"
    "v_cvt_pk_f16_f32 v34, v52, v55\n"
    "v_cvt_pk_f16_f32 v42, -v53, -v56\n"
    "v_cvt_pk_f16_f32 v35, v58, v61\n"
    "v_cvt_pk_f16_f32 v43, -v59, -v62\n"
    "v_cvt_pk_f16_f32 v36, v63, v66\n"
    "v_cvt_pk_f16_f32 v44, -v64, -v67\n"
    "v_cvt_pk_f16_f32 v37, v69, v72\n"
    "v_cvt_pk_f16_f32 v45, -v70, -v73\n"
    "v_mul_f32 v76, v74, v28\n"
    "v_mul_f32 v77, v74, v75\n"
    "v_mul_u32_u24 v79, 0x202, v79\n"
    "v_and_b32_e64 v80, 16, v4\n"
    "s_add_u32 s40, s8, 0x80800\n"
    "v_lshl_or_b32 v51, v51, 16, v51\n"
    "v_xor_b32 v38, v34, v50\n"
    "v_xor_b32 v46, v42, v50\n"
    "v_xor_b32 v39, v35, v50\n"
    "v_xor_b32 v47, v43, v50\n"
    "v_xor_b32 v40, v36, v50\n"
    "v_xor_b32 v48, v44, v50\n"
    "v_xor_b32 v41, v37, v50\n"
    "v_xor_b32 v49, v45, v50\n"
    "v_mul_f32 v76, 0x3b000000, v76\n"
    "v_mul_f32 v77, 0x3c800000, v77\n"
    "v_add3_u32 v78, v79, v80, s30\n"
    "s_addc_u32 s41, s9, 0\n"
    "s_bitcmp1_b32 s27, 0\n"
    "s_cselect_b64 s[20:21], s[12:13], s[10:11]\n"
    "v_bfe_u32 v26, v0, 2, 2\n"
    "v_lshl_add_u32 v26, v15, 3, v26\n"
    "v_and_b32 v27, 3, v0\n"
    "v_min_u32 v27, 2, v27\n"
    "s_mul_i32 s28, s25, 3\n"
    "s_add_u32 s28, s28, 0x80\n"
    "v_add_u32 v27, s28, v27\n"
    "v_min_u32 v27, 0xab, v27\n"
    "v_mul_u32_u24 v26, 0xac, v26\n"
    "v_add_lshl_u32 v26, v26, v27, 4\n"
    "s_lshr_b32 s29, s27, 1\n"
    "s_lshl3_add_u32 s29, s24, s29\n"
    "s_mul_i32 s29, s29, 0x2b00\n"
    "s_add_u32 s20, s20, s29\n"
    "s_addc_u32 s21, s21, 0\n"
    "s_add_u32 m0, s37, 0x24000\n"
    "s_nop 0\n"
    "global_load_lds_dwordx4 v26, s[20:21]\n"
    "v_fmamk_f32 v29, v28, 0x0, v76\n"
    "v_fmamk_f32 v71, v28, 0x3b000000, v76\n"
    "v_fmamk_f32 v79, v28, 0x3b800000, v76\n"
    "v_fmamk_f32 v82, v28, 0x3bc00000, v76\n"
    "v_fmamk_f32 v85, v28, 0x3d000000, v76\n"
    "v_fmamk_f32 v88, v28, 0x3d080000, v76\n"
    "v_fmamk_f32 v91, v28, 0x3d100000, v76\n"
    "v_fmamk_f32 v94, v28, 0x3d180000, v76\n"
    "v_fmamk_f32 v97, v28, 0x3d800000, v76\n"
    "v_fmamk_f32 v100, v28, 0x3d840000, v76\n"
    "v_fmamk_f32 v103, v28, 0x3d880000, v76\n"
    "v_fmamk_f32 v106, v28, 0x3d8c0000, v76\n"
    "v_fmamk_f32 v109, v28, 0x3dc00000, v76\n"
    "v_fmamk_f32 v112, v28, 0x3dc40000, v76\n"
    "v_fmamk_f32 v115, v28, 0x3dc80000, v76\n"
    "v_fmamk_f32 v118, v28, 0x3dcc0000, v76\n"
    "v_cos_f32 v50, v29\n"
    "v_sin_f32 v70, v29\n"
    "v_cos_f32 v72, v71\n"
    "v_sin_f32 v73, v71\n"
    "v_cos_f32 v80, v79\n"
    "v_sin_f32 v81, v79\n"
    "v_cos_f32 v83, v82\n"
    "v_sin_f32 v84, v82\n"
    "v_cos_f32 v86, v85\n"
    "v_sin_f32 v87, v85\n"
    "v_cos_f32 v89, v88\n"
    "v_sin_f32 v90, v88\n"
    "v_cos_f32 v92, v91\n"
    "v_sin_f32 v93, v91\n"
    "v_cos_f32 v95, v94\n"
    "v_sin_f32 v96, v94\n"
    "v_cos_f32 v98, v97\n"
    "v_sin_f32 v99, v97\n"
    "v_cos_f32 v101, v100\n"
    "v_sin_f32 v102, v100\n"
    "v_cos_f32 v104, v103\n"
    "v_sin_f32 v105, v103\n"
    "v_cos_f32 v107, v106\n"
    "v_sin_f32 v108, v106\n"
    "v_cos_f32 v110, v109\n"
    "v_sin_f32 v111, v109\n"
    "v_cos_f32 v113, v112\n"
    "v_sin_f32 v114, v112\n"
    "v_cos_f32 v116, v115\n"
    "v_sin_f32 v117, v115\n"
    "v_cos_f32 v119, v118\n"
    "v_sin_f32 v120, v118\n"
    "v_cvt_pk_f16_f32 v54, v50, v70\n"
    "v_cvt_pk_f16_f32 v55, v72, v73\n"
    "v_cvt_pk_f16_f32 v56, v80, v81\n"
    "v_cvt_pk_f16_f32 v57, v83, v84\n"
    "v_cvt_pk_f16_f32 v58, v86, v87\n"
    "v_cvt_pk_f16_f32 v59, v89, v90\n"
    "v_cvt_pk_f16_f32 v60, v92, v93\n"
    "v_cvt_pk_f16_f32 v61, v95, v96\n"
    "v_cvt_pk_f16_f32 v62, v98, v99\n"
    "v_cvt_pk_f16_f32 v63, v101, v102\n"
    "v_cvt_pk_f16_f32 v64, v104, v105\n"
    "v_cvt_pk_f16_f32 v65, v107, v108\n"
    "v_cvt_pk_f16_f32 v66, v110, v111\n"
    "v_cvt_pk_f16_f32 v67, v113, v114\n"
    "v_cvt_pk_f16_f32 v68, v116, v117\n"
    "v_cvt_pk_f16_f32 v69, v119, v120\n"
    "v_lshrrev_b32_e64 v164, 5, v0\n"
    "v_bfe_u32 v168, v0, 3, 2\n"
    "v_fmamk_f32 v29, v75, 0x0, v77\n"
    "v_fmamk_f32 v140, v75, 0x3c800000, v77\n"
    "v_fmamk_f32 v143, v75, 0x3d000000, v77\n"
    "v_fmamk_f32 v146, v75, 0x3d400000, v77\n"
    "v_fmamk_f32 v149, v75, 0x3e800000, v77\n"
    "v_fmamk_f32 v152, v75, 0x3e880000, v77\n"
    "v_fmamk_f32 v155, v75, 0x3e900000, v77\n"
    "v_fmamk_f32 v158, v75, 0x3e980000, v77\n"
    "v_lshl_add_u32 v165, v164, 3, v168\n"
    "v_cos_f32 v50, v29\n"
    "v_sin_f32 v79, v29\n"
    "v_cos_f32 v141, v140\n"
    "v_sin_f32 v142, v140\n"
    "v_cos_f32 v144, v143\n"
    "v_sin_f32 v145, v143\n"
    "v_cos_f32 v147, v146\n"
    "v_sin_f32 v148, v146\n"
    "v_cos_f32 v150, v149\n"
    "v_sin_f32 v151, v149\n"
    "v_cos_f32 v153, v152\n"
    "v_sin_f32 v154, v152\n"
    "v_cos_f32 v156, v155\n"
    "v_sin_f32 v157, v155\n"
    "v_cos_f32 v159, v158\n"
    "v_sin_f32 v160, v158\n"
    "v_lshlrev_b32_e64 v168, 5, v168\n"
    "v_and_b32 v169, 0x1f0, v4\n"
    "v_lshrrev_b32 v167, 2, v5\n"
    "v_and_b32 v170, 3, v0\n"
    "v_xor_b32_e64 v168, v168, v169\n"
    "v_mul_u32_u24 v165, 0xa0, v165\n"
    "v_and_b32 v166, 0x70, v4\n"
    "s_mov_b32 s30, 0x25800\n"
    "v_mul_u32_u24 v162, 0xa0, v167\n"
    "v_lshlrev_b32_e64 v170, 3, v170\n"
    "v_lshl_add_u32 v163, v164, 10, v168\n"
    "v_cvt_pk_f16_f32 v70, v50, v79\n"
    "v_cvt_pk_f16_f32 v108, -v79, v50\n"
    "v_cvt_pk_f16_f32 v80, v50, v79\n"
    "v_cvt_pk_f16_f32 v112, -v79, v50\n"
    "v_cvt_pk_f16_f32 v71, v141, v142\n"
    "v_cvt_pk_f16_f32 v109, -v142, v141\n"
    "v_cvt_pk_f16_f32 v81, -v142, v141\n"
    "v_cvt_pk_f16_f32 v113, -v141, -v142\n"
    "v_cvt_pk_f16_f32 v72, v144, v145\n"
    "v_cvt_pk_f16_f32 v110, -v145, v144\n"
    "v_cvt_pk_f16_f32 v82, -v144, -v145\n"
    "v_cvt_pk_f16_f32 v114, v145, -v144\n"
    "v_cvt_pk_f16_f32 v73, v147, v148\n"
    "v_cvt_pk_f16_f32 v111, -v148, v147\n"
    "v_cvt_pk_f16_f32 v83, v148, -v147\n"
    "v_cvt_pk_f16_f32 v115, v147, v148\n"
    "v_cvt_pk_f16_f32 v84, v150, v151\n"
    "v_cvt_pk_f16_f32 v116, -v151, v150\n"
    "v_cvt_pk_f16_f32 v88, v150, v151\n"
    "v_cvt_pk_f16_f32 v120, -v151, v150\n"
    "v_cvt_pk_f16_f32 v85, v153, v154\n"
    "v_cvt_pk_f16_f32 v117, -v154, v153\n"
    "v_cvt_pk_f16_f32 v89, -v154, v153\n"
    "v_cvt_pk_f16_f32 v121, -v153, -v154\n"
    "v_cvt_pk_f16_f32 v86, v156, v157\n"
    "v_cvt_pk_f16_f32 v118, -v157, v156\n"
    "v_cvt_pk_f16_f32 v90, -v156, -v157\n"
    "v_cvt_pk_f16_f32 v122, v157, -v156\n"
    "v_cvt_pk_f16_f32 v87, v159, v160\n"
    "v_cvt_pk_f16_f32 v119, -v160, v159\n"
    "v_cvt_pk_f16_f32 v91, v160, -v159\n"
    "v_cvt_pk_f16_f32 v123, v159, v160\n"
    "v_add3_u32 v161, v165, v166, s30\n"
    "v_add3_u32 v162, v162, v170, s30\n"
    "v_add_u32 v163, 0x20000, v163\n"
    "s_mov_b32 s42, 0x3f803f80\n"
    "v_mov_b32 v171, 0\n"
    "v_mov_b32 v172, 0\n"
    "v_mov_b32 v173, 0\n"
    "v_mov_b32 v174, 0\n"
    "s_add_u32 s68, s24, s25\n"
    "s_lshl_b32 s68, s68, 1\n"
    "s_cmp_ge_u32 s27, 2\n"
    "s_cselect_b64 s[60:61], s[12:13], s[10:11]\n"
    "s_and_b32 s61, s61, 0xffff\n"
    "s_and_b32 s43, s27, 1\n"
    "s_mul_i32 s43, s43, 0x2b00\n"
    "s_lshl_b32 s58, s25, 7\n"
    "s_add_u32 s43, s43, s58\n"
    "s_add_u32 s69, s68, 0\n"
    "s_and_b32 s69, s69, 3\n"
    "s_add_u32 s70, s68, 1\n"
    "v_bfe_u32 v77, v0, 2, 2\n"
    "v_lshlrev_b32 v79, 1, v25\n"
    "s_and_b32 s70, s70, 3\n"
    "v_lshl_add_u32 v77, v15, 3, v77\n"
    "v_xor_b32 v79, v79, v0\n"
    "s_add_u32 s71, s68, 2\n"
    "v_mul_u32_u24 v77, 0xac0, v77\n"
    "v_and_b32_e64 v79, 3, v79\n"
    "s_and_b32 s71, s71, 3\n"
    "v_lshl_add_u32 v76, v79, 4, v77\n"
    "s_add_u32 s72, s68, 3\n"
    "v_add_u32 v76, s43, v76\n"
    "s_and_b32 s72, s72, 3\n"
    "s_lshl2_add_u32 s43, s2, s27\n"
    "s_mul_i32 s43, s43, 3\n"
    "s_add_u32 s43, s43, 0x2000\n"
    "s_min_u32 s43, s43, 0x2afd\n"
    "v_min_u32_e64 v144, 2, v5\n"
    "v_add_lshl_u32 v144, v144, s43, 7\n"
    "v_and_b32_e64 v145, 48, v76\n"
    "s_mov_b32 s62, 0x158000\n"
    "global_load_dword v18, v144, s[14:15]\n"
    "s_mov_b32 s63, 0x20000\n"
    "v_add_u32_e64 v145, v145, v76\n"
    "s_mul_i32 s43, s24, 0x15800\n"
    "s_mul_i32 s69, s69, 0x56000\n"
    "s_mul_i32 s70, s70, 0x56000\n"
    "s_mul_i32 s71, s71, 0x56000\n"
    "s_mul_i32 s72, s72, 0x56000\n"
    "buffer_load_dword v20, v145, s[60:63], s43 offen\n"
    "s_lshl_b32 s43, s26, 13\n"
    "s_add_u32 s43, s43, 0x411000\n"
    "s_add_u32 s52, s18, s43\n"
    "s_addc_u32 s53, s19, 0\n"
    "s_lshl2_add_u32 s43, s24, s27\n"
    "s_lshl_b32 s43, s43, 5\n"
    "s_add_u32 s43, s43, s25\n"
    "s_lshl_b32 s43, s43, 10\n"
    "s_add_u32 s54, s18, s43\n"
    "v_mov_b32 v140, -1\n"
    "v_mov_b32 v141, -1\n"
    "v_mov_b32 v142, -1\n"
    "v_mov_b32 v143, -1\n"
    "s_addc_u32 s55, s19, 0\n"
    "s_waitcnt vmcnt(4)\n"
    "ds_write_b128 v163, v[6:9]\n"
    "ds_write_b128 v163, v[10:13] offset:512\n"
    "v_dot2c_f32_bf16 v171, s42, v6\n"
    "v_dot2c_f32_bf16 v172, v6, v6\n"
    "v_dot2c_f32_bf16 v173, s42, v10\n"
    "v_dot2c_f32_bf16 v174, v10, v10\n"
    "v_dot2c_f32_bf16 v171, s42, v7\n"
    "v_dot2c_f32_bf16 v172, v7, v7\n"
    "v_dot2c_f32_bf16 v173, s42, v11\n"
    "v_dot2c_f32_bf16 v174, v11, v11\n"
    "v_dot2c_f32_bf16 v171, s42, v8\n"
    "v_dot2c_f32_bf16 v172, v8, v8\n"
    "v_dot2c_f32_bf16 v173, s42, v12\n"
    "v_dot2c_f32_bf16 v174, v12, v12\n"
    "v_dot2c_f32_bf16 v171, s42, v9\n"
    "v_dot2c_f32_bf16 v172, v9, v9\n"
    "v_dot2c_f32_bf16 v173, s42, v13\n"
    "v_dot2c_f32_bf16 v174, v13, v13\n"
    "v_lshlrev_b32_e64 v158, 16, v6\n"
    "v_and_b32 v159, 0xffff0000, v6\n"
    "v_add_f32 v171, v171, v173\n"
    "v_add_f32 v172, v172, v174\n"
    "v_lshlrev_b32_e64 v164, 16, v7\n"
    "v_add_f32_dpp v171, v171, v171 quad_perm:[1,0,3,2] row_mask:0xf bank_mask:0xf bound_ctrl:1\n"
    "v_add_f32_dpp v172, v172, v172 quad_perm:[1,0,3,2] row_mask:0xf bank_mask:0xf bound_ctrl:1\n"
    "v_and_b32 v165, 0xffff0000, v7\n"
    "v_add_f32_dpp v171, v171, v171 quad_perm:[2,3,0,1] row_mask:0xf bank_mask:0xf bound_ctrl:1\n"
    "v_add_f32_dpp v172, v172, v172 quad_perm:[2,3,0,1] row_mask:0xf bank_mask:0xf bound_ctrl:1\n"
    "v_lshlrev_b32_e64 v166, 16, v8\n"
    "v_add_f32_dpp v171, v171, v171 row_half_mirror row_mask:0xf bank_mask:0xf bound_ctrl:1\n"
    "v_add_f32_dpp v172, v172, v172 row_half_mirror row_mask:0xf bank_mask:0xf bound_ctrl:1\n"
    "v_and_b32 v167, 0xffff0000, v8\n"
    "v_add_f32_dpp v171, v171, v171 row_mirror row_mask:0xf bank_mask:0xf bound_ctrl:1\n"
    "v_add_f32_dpp v172, v172, v172 row_mirror row_mask:0xf bank_mask:0xf bound_ctrl:1\n"
    "v_mov_b32 v77, v171\n"
    "v_mov_b32 v79, v172\n"
    "v_lshlrev_b32 v168, 16, v9\n"
    "v_permlane16_swap_b32 v171, v77\n"
    "v_permlane16_swap_b32 v172, v79\n"
    "v_add_f32 v171, v171, v77\n"
    "v_add_f32_e64 v172, v172, v79\n"
    "v_mul_f32 v144, 0x3b000000, v171\n"
    "v_mul_f32 v145, 0x3b000000, v172\n"
    "v_fma_f32 v145, -v144, v144, v145\n"
    "v_max_f32_e64 v145, 0, v145\n"
    "v_add_f32 v145, 0x3727c5ac, v145\n"
    "v_rsq_f32_e64 v145, v145\n"
    "v_and_b32 v169, 0xffff0000, v9\n"
    "v_mul_f32 v146, 0x3c800000, v145\n"
    "v_mul_f32_e64 v148, -v144, v146\n"
    "v_pk_fma_f32 v[158:159], v[158:159], v[146:147], v[148:149] op_sel_hi:[1,0,0]\n"
    "v_pk_fma_f32 v[164:165], v[164:165], v[146:147], v[148:149] op_sel_hi:[1,0,0]\n"
    "v_pk_fma_f32 v[166:167], v[166:167], v[146:147], v[148:149] op_sel_hi:[1,0,0]\n"
    "v_pk_fma_f32 v[168:169], v[168:169], v[146:147], v[148:149] op_sel_hi:[1,0,0]\n"
    "v_cvt_pk_f16_f32 v150, v158, v159\n"
    "v_cvt_pk_f16_f32 v151, v164, v165\n"
    "v_cvt_pk_f16_f32 v152, v166, v167\n"
    "v_cvt_pk_f16_f32 v153, v168, v169\n"
    "v_lshlrev_b32_e64 v176, 16, v10\n"
    "v_and_b32 v177, 0xffff0000, v10\n"
    "v_lshlrev_b32_e64 v178, 16, v11\n"
    "v_and_b32 v179, 0xffff0000, v11\n"
    "v_lshlrev_b32_e64 v180, 16, v12\n"
    "v_and_b32 v181, 0xffff0000, v12\n"
    "v_lshlrev_b32_e64 v182, 16, v13\n"
    "v_and_b32 v183, 0xffff0000, v13\n"
    "v_pk_fma_f32 v[176:177], v[176:177], v[146:147], v[148:149] op_sel_hi:[1,0,0]\n"
    "v_pk_fma_f32 v[178:179], v[178:179], v[146:147], v[148:149] op_sel_hi:[1,0,0]\n"
    "v_pk_fma_f32 v[180:181], v[180:181], v[146:147], v[148:149] op_sel_hi:[1,0,0]\n"
    "v_pk_fma_f32 v[182:183], v[182:183], v[146:147], v[148:149] op_sel_hi:[1,0,0]\n"
    "v_cvt_pk_f16_f32 v154, v176, v177\n"
    "v_cvt_pk_f16_f32 v155, v178, v179\n"
    "v_cvt_pk_f16_f32 v156, v180, v181\n"
    "v_cvt_pk_f16_f32 v157, v182, v183\n"
    "ds_write_b128 v161, v[150:153]\n"
    "ds_write_b128 v161, v[154:157] offset:640\n"
    "v_xor_b32 v92, v70, v51\n"
    "v_xor_b32 v124, v108, v51\n"
    "v_xor_b32 v93, v71, v51\n"
    "v_xor_b32 v125, v109, v51\n"
    "v_xor_b32 v94, v72, v51\n"
    "v_xor_b32 v126, v110, v51\n"
    "v_xor_b32 v95, v73, v51\n"
    "v_xor_b32 v127, v111, v51\n"
    "v_xor_b32 v96, v80, v51\n"
    "v_xor_b32 v128, v112, v51\n"
    "v_xor_b32 v97, v81, v51\n"
    "v_xor_b32 v129, v113, v51\n"
    "v_xor_b32 v98, v82, v51\n"
    "v_xor_b32 v130, v114, v51\n"
    "v_xor_b32 v99, v83, v51\n"
    "v_xor_b32 v131, v115, v51\n"
    "s_waitcnt lgkmcnt(0)\n"
    "s_barrier\n"
    "ds_read_b64_tr_b16 v[8:9], v162 offset:2560\n"
    "ds_read_b64_tr_b16 v[6:7], v162 offset:0\n"
    "ds_read_b64_tr_b16 v[10:11], v162 offset:5120\n"
    "ds_read_b64_tr_b16 v[12:13], v162 offset:7680\n"
    "ds_read_b64_tr_b16 v[146:147], v162 offset:2592\n"
    "ds_read_b64_tr_b16 v[144:145], v162 offset:32\n"
    "ds_read_b64_tr_b16 v[152:153], v162 offset:64\n"
    "ds_read_b64_tr_b16 v[154:155], v162 offset:2624\n"
    "ds_read_b64_tr_b16 v[148:149], v162 offset:5152\n"
    "ds_read_b64_tr_b16 v[150:151], v162 offset:7712\n"
    "ds_read_b64_tr_b16 v[158:159], v162 offset:7744\n"
    "ds_read_b64_tr_b16 v[156:157], v162 offset:5184\n"
    "ds_read_b64_tr_b16 v[166:167], v162 offset:2656\n"
    "ds_read_b64_tr_b16 v[164:165], v162 offset:96\n"
    "ds_read_b64_tr_b16 v[174:175], v162 offset:7776\n"
    "ds_read_b64_tr_b16 v[172:173], v162 offset:5216\n"
    "s_lshl_b32 s28, s27, 19\n"
    "s_lshl_b32 s29, s26, 8\n"
    "s_add_u32 s28, s28, s29\n"
    "s_add_u32 s28, s28, 0x10000\n"
    "s_waitcnt lgkmcnt(14)\n"
    "v_mfma_f32_16x16x32_f16 v[196:199], v[6:9], v[42:45], 0\n"
    "s_add_u32 s20, s18, s28\n"
    "s_addc_u32 s21, s19, 0\n"
    "s_waitcnt lgkmcnt(12)\n"
    "v_mfma_f32_16x16x32_f16 v[192:195], v[6:9], v[34:37], 0\n"
    "s_add_u32 s74, s20, 0x40000\n"
    "s_addc_u32 s75, s21, 0\n"
    "s_waitcnt lgkmcnt(10)\n"
    "v_mfma_f32_16x16x32_f16 v[192:195], v[10:13], v[38:41], v[192:195]\n"
    "s_add_u32 s28, s34, 0\n"
    "s_lshl_b32 s29, s28, 14\n"
    "s_waitcnt lgkmcnt(8)\n"
    "v_mfma_f32_16x16x32_f16 v[196:199], v[10:13], v[46:49], v[196:199]\n"
    "s_and_b32 s28, s28, 3\n"
    "s_or_b32 s28, s28, s29\n"
    "s_waitcnt lgkmcnt(6)\n"
    "v_mfma_f32_16x16x32_f16 v[200:203], v[144:147], v[34:37], 0\n"
    "s_and_b32 s73, s28, 0x30003\n"
    "s_add_u32 s28, s34, 1\n"
    "s_waitcnt lgkmcnt(4)\n"
    "v_mfma_f32_16x16x32_f16 v[204:207], v[144:147], v[42:45], 0\n"
    "s_lshl_b32 s29, s28, 14\n"
    "v_cvt_pk_f16_f32 v196, v192, v196\n"
    "v_cvt_pk_f16_f32 v199, v195, v199\n"
    "v_mfma_f32_16x16x32_f16 v[212:215], v[152:155], v[42:45], 0\n"
    "s_waitcnt lgkmcnt(2)\n"
    "v_cvt_pk_f16_f32 v198, v194, v198\n"
    "v_cvt_pk_f16_f32 v197, v193, v197\n"
    "v_mfma_f32_16x16x32_f16 v[208:211], v[152:155], v[34:37], 0\n"
    "s_waitcnt lgkmcnt(0)\n"
    "v_pk_mul_f16 v195, v199, v57 op_sel:[0,0] op_sel_hi:[1,0]\n"
    "v_pk_mul_f16 v192, v196, v54 op_sel:[0,0] op_sel_hi:[1,0]\n"
    "v_mfma_f32_16x16x32_f16 v[200:203], v[148:151], v[38:41], v[200:203]\n"
    "s_and_b32 s28, s28, 3\n"
    "v_pk_mul_f16 v194, v198, v56 op_sel:[0,0] op_sel_hi:[1,0]\n"
    "v_pk_fma_f16 v196, v196, v54, v192 op_sel:[1,1,0] op_sel_hi:[0,1,1] neg_hi:[0,1,0]\n"
    "v_mfma_f32_16x16x32_f16 v[204:207], v[148:151], v[46:49], v[204:207]\n"
    "s_or_b32 s28, s28, s29\n"
    "v_pk_mul_f16 v193, v197, v55 op_sel:[0,0] op_sel_hi:[1,0]\n"
    "v_pk_fma_f16 v198, v198, v56, v194 op_sel:[1,1,0] op_sel_hi:[0,1,1] neg_hi:[0,1,0]\n"
    "v_mfma_f32_16x16x32_f16 v[208:211], v[156:159], v[38:41], v[208:211]\n"
    "s_and_b32 s76, s28, 0x30003\n"
    "v_pk_fma_f16 v199, v199, v57, v195 op_sel:[1,1,0] op_sel_hi:[0,1,1] neg_hi:[0,1,0]\n"
    "v_pk_fma_f16 v197, v197, v55, v193 op_sel:[1,1,0] op_sel_hi:[0,1,1] neg_hi:[0,1,0]\n"
    "v_mfma_f32_16x16x32_f16 v[212:215], v[156:159], v[46:49], v[212:215]\n"
    "s_add_u32 s28, s34, 2\n"
    "v_cvt_pk_f16_f32 v207, v203, v207\n"
    "v_cvt_pk_f16_f32 v206, v202, v206\n"
    "v_mfma_f32_16x16x32_f16 v[220:223], v[164:167], v[42:45], 0\n"
    "s_lshl_b32 s29, s28, 14\n"
    "v_cvt_pk_f16_f32 v204, v200, v204\n"
    "v_cvt_pk_f16_f32 v205, v201, v205\n"
    "v_mfma_f32_16x16x32_f16 v[216:219], v[164:167], v[34:37], 0\n"
    "s_and_b32 s28, s28, 3\n"
    "v_pk_mul_f16 v203, v207, v61 op_sel:[0,0] op_sel_hi:[1,0]\n"
    "v_pk_mul_f16 v202, v206, v60 op_sel:[0,0] op_sel_hi:[1,0]\n"
    "v_mfma_f32_16x16x32_f16 v[216:219], v[172:175], v[38:41], v[216:219]\n"
    "s_or_b32 s28, s28, s29\n"
    "v_pk_mul_f16 v201, v205, v59 op_sel:[0,0] op_sel_hi:[1,0]\n"
    "v_cvt_pk_f16_f32 v214, v210, v214\n"
    "v_mfma_f32_16x16x32_f16 v[220:223], v[172:175], v[46:49], v[220:223]\n"
    "s_and_b32 s77, s28, 0x30003\n"
    "v_cvt_pk_f16_f32 v215, v211, v215\n"
    "v_pk_fma_f16 v207, v207, v61, v203 op_sel:[1,1,0] op_sel_hi:[0,1,1] neg_hi:[0,1,0]\n"
    "v_mfma_f32_16x16x32_f16 v[184:187], v[196:199], v[108:111], 0\n"
    "s_add_u32 s28, s34, 3\n"
    "v_pk_mul_f16 v200, v204, v58 op_sel:[0,0] op_sel_hi:[1,0]\n"
    "v_pk_fma_f16 v206, v206, v60, v202 op_sel:[1,1,0] op_sel_hi:[0,1,1] neg_hi:[0,1,0]\n"
    "v_pk_fma_f16 v204, v204, v58, v200 op_sel:[1,1,0] op_sel_hi:[0,1,1] neg_hi:[0,1,0]\n"
    "v_pk_fma_f16 v205, v205, v59, v201 op_sel:[1,1,0] op_sel_hi:[0,1,1] neg_hi:[0,1,0]\n"
    "v_cvt_pk_f16_f32 v213, v209, v213\n"
    "v_cvt_pk_f16_f32 v212, v208, v212\n"
    "v_mfma_f32_16x16x32_f16 v[176:179], v[196:199], v[70:73], 0\n"
    "s_lshl_b32 s29, s28, 14\n"
    "v_pk_mul_f16 v210, v214, v64 op_sel:[0,0] op_sel_hi:[1,0]\n"
    "v_pk_mul_f16 v211, v215, v65 op_sel:[0,0] op_sel_hi:[1,0]\n"
    "v_pk_mul_f16 v209, v213, v63 op_sel:[0,0] op_sel_hi:[1,0]\n"
    "v_pk_mul_f16 v208, v212, v62 op_sel:[0,0] op_sel_hi:[1,0]\n"
    "v_pk_fma_f16 v215, v215, v65, v211 op_sel:[1,1,0] op_sel_hi:[0,1,1] neg_hi:[0,1,0]\n"
    "v_pk_fma_f16 v214, v214, v64, v210 op_sel:[1,1,0] op_sel_hi:[0,1,1] neg_hi:[0,1,0]\n"
    "v_mfma_f32_16x16x32_f16 v[184:187], v[204:207], v[116:119], v[184:187]\n"
    "s_and_b32 s28, s28, 3\n"
    "v_pk_fma_f16 v212, v212, v62, v208 op_sel:[1,1,0] op_sel_hi:[0,1,1] neg_hi:[0,1,0]\n"
    "v_pk_fma_f16 v213, v213, v63, v209 op_sel:[1,1,0] op_sel_hi:[0,1,1] neg_hi:[0,1,0]\n"
    "v_cvt_pk_f16_f32 v220, v216, v220\n"
    "v_mfma_f32_16x16x32_f16 v[176:179], v[204:207], v[84:87], v[176:179]\n"
    "s_or_b32 s28, s28, s29\n"
    "v_cvt_pk_f16_f32 v223, v219, v223\n"
    "v_cvt_pk_f16_f32 v222, v218, v222\n"
    "v_cvt_pk_f16_f32 v221, v217, v221\n"
    "v_pk_mul_f16 v216, v220, v66 op_sel:[0,0] op_sel_hi:[1,0]\n"
    "v_mfma_f32_16x16x32_f16 v[184:187], v[212:215], v[124:127], v[184:187]\n"
    "s_and_b32 s78, s28, 0x30003\n"
    "v_pk_mul_f16 v217, v221, v67 op_sel:[0,0] op_sel_hi:[1,0]\n"
    "v_pk_mul_f16 v219, v223, v69 op_sel:[0,0] op_sel_hi:[1,0]\n"
    "v_pk_mul_f16 v218, v222, v68 op_sel:[0,0] op_sel_hi:[1,0]\n"
    "v_xor_b32_e64 v132, v116, v51\n"
    "v_pk_fma_f16 v221, v221, v67, v217 op_sel:[1,1,0] op_sel_hi:[0,1,1] neg_hi:[0,1,0]\n"
    "v_xor_b32 v100, v84, v51\n"
    "v_xor_b32 v101, v85, v51\n"
    "v_mfma_f32_16x16x32_f16 v[188:191], v[196:199], v[112:115], 0\n"
    "v_xor_b32_e64 v135, v119, v51\n"
    "v_pk_fma_f16 v220, v220, v66, v216 op_sel:[1,1,0] op_sel_hi:[0,1,1] neg_hi:[0,1,0]\n"
    "v_pk_fma_f16 v222, v222, v68, v218 op_sel:[1,1,0] op_sel_hi:[0,1,1] neg_hi:[0,1,0]\n"
    "v_pk_fma_f16 v223, v223, v69, v219 op_sel:[1,1,0] op_sel_hi:[0,1,1] neg_hi:[0,1,0]\n"
    "v_xor_b32_e64 v133, v117, v51\n"
    "v_mfma_f32_16x16x32_f16 v[176:179], v[212:215], v[92:95], v[176:179]\n"
    "v_xor_b32 v134, v118, v51\n"
    "v_xor_b32 v103, v87, v51\n"
    "v_mfma_f32_16x16x32_f16 v[180:183], v[196:199], v[80:83], 0\n"
    "v_xor_b32 v102, v86, v51\n"
    "v_xor_b32 v136, v120, v51\n"
    "v_mfma_f32_16x16x32_f16 v[184:187], v[220:223], v[132:135], v[184:187]\n"
    "v_xor_b32 v139, v123, v51\n"
    "v_xor_b32 v137, v121, v51\n"
    "v_mfma_f32_16x16x32_f16 v[188:191], v[204:207], v[120:123], v[188:191]\n"
    "v_xor_b32 v138, v122, v51\n"
    "v_xor_b32 v105, v89, v51\n"
    "v_mfma_f32_16x16x32_f16 v[176:179], v[220:223], v[100:103], v[176:179]\n"
    "v_xor_b32 v106, v90, v51\n"
    "v_xor_b32 v107, v91, v51\n"
    "v_mfma_f32_16x16x32_f16 v[180:183], v[204:207], v[88:91], v[180:183]\n"
    "v_xor_b32 v104, v88, v51\n"
    "v_mov_b32 v77, 0x30003\n"
    "v_mfma_f32_16x16x32_f16 v[188:191], v[212:215], v[128:131], v[188:191]\n"
    "s_nop 1\n"
    "v_cvt_pk_f16_f32 v179, v179, v187\n"
    "v_cvt_pk_f16_f32 v176, v176, v184\n"
    "v_mfma_f32_16x16x32_f16 v[180:183], v[212:215], v[96:99], v[180:183]\n"
    "v_cvt_pk_f16_f32 v178, v178, v186\n"
    "v_cvt_pk_f16_f32 v177, v177, v185\n"
    "v_mfma_f32_16x16x32_f16 v[188:191], v[220:223], v[136:139], v[188:191]\n"
    "v_add_u32 v179, 0x20002, v179\n"
    "v_add_u32 v176, 0x20002, v176\n"
    "v_mfma_f32_16x16x32_f16 v[180:183], v[220:223], v[104:107], v[180:183]\n"
    "v_add_u32 v177, 0x20002, v177\n"
    "v_add_u32 v178, 0x20002, v178\n"
    "v_lshlrev_b32_e64 v2, 4, v14\n"
    "v_bfi_b32 v179, v77, s78, v179\n"
    "v_bfi_b32 v177, v77, s76, v177\n"
    "v_bfi_b32 v178, v77, s77, v178\n"
    "v_bfi_b32 v176, v77, s73, v176\n"
    "v_lshl_add_u32 v2, v15, 16, v2\n"
    "v_cvt_pk_f16_f32 v180, v180, v188\n"
    "v_cvt_pk_f16_f32 v182, v182, v190\n"
    "v_cvt_pk_f16_f32 v183, v183, v191\n"
    "v_cvt_pk_f16_f32 v181, v181, v189\n"
    "v_add_u32 v180, 0x20002, v180\n"
    "v_add_u32 v182, 0x20002, v182\n"
    "v_add_u32 v181, 0x20002, v181\n"
    "v_bfi_b32 v180, v77, s73, v180\n"
    "v_add_u32 v183, 0x20002, v183\n"
    "v_bfi_b32 v182, v77, s77, v182\n"
    "v_bfi_b32 v181, v77, s76, v181\n"
    "v_bfi_b32 v183, v77, s78, v183\n"
    "global_store_dwordx4 v2, v[176:179], s[20:21] sc1\n"
    "global_store_dwordx4 v2, v[180:183], s[74:75] sc1\n"
    "s_nop 0\n"
    "s_and_b32 s43, s26, 7\n"
    "s_lshl_b32 s43, s43, 7\n"
    "s_add_u32 s44, s14, s43\n"
    "s_addc_u32 s45, s15, 0\n"
    "s_and_b32 s45, s45, 0xffff\n"
    "s_sub_u32 s46, 0x158000, s43\n"
    "s_mov_b32 s59, 0\n"
    "s_and_b32 s59, s59, 1\n"
    "s_lshl_b32 s59, s59, 18\n"
    "s_lshr_b32 s43, s22, 0\n"
    "s_add_u32 s64, s43, 0\n"
    "s_and_b32 s64, s64, 3\n"
    "s_mul_i32 s64, s64, 0x3000\n"
    "s_add_u32 s64, s64, s37\n"
    "s_add_u32 s65, s43, 1\n"
    "s_and_b32 s65, s65, 3\n"
    "s_mul_i32 s65, s65, 0x3000\n"
    "s_add_u32 s65, s65, s37\n"
    "s_add_u32 s66, s43, 2\n"
    "s_and_b32 s66, s66, 3\n"
    "v_and_b32 v107, 3, v0\n"
    "s_mul_i32 s66, s66, 0x3000\n"
    "global_load_dwordx2 v[26:27], v78, s[6:7]\n"
    "v_xor_b32 v107, s27, v107\n"
    "s_add_u32 s66, s66, s37\n"
    "v_lshlrev_b32 v107, 5, v107\n"
    "s_add_u32 s67, s43, 3\n"
    "v_lshl_add_u32 v107, v15, 3, v107\n"
    "v_and_b32 v108, 7, v0\n"
    "s_and_b32 s67, s67, 3\n"
    "v_lshl_add_u32 v107, v108, 7, v107\n"
    "s_mul_i32 s67, s67, 0x3000\n"
    "v_lshl_add_u32 v107, v23, 10, v107\n"
    "s_add_u32 s67, s67, s37\n"
    "v_add_u32 v106, 0x20000, v107\n"
    "s_and_b32 s30, s22, 6\n"
    "ds_read_b64 v[112:113], v106 offset:0\n"
    "ds_read_b64 v[116:117], v106 offset:2048\n"
    "ds_read_b64 v[120:121], v106 offset:4096\n"
    "ds_read_b64 v[124:125], v106 offset:6144\n"
    "s_lshr_b32 s31, s23, 4\n"
    "s_add_u32 s30, s30, s31\n"
    "global_load_dwordx2 v[30:31], v78, s[8:9]\n"
    "s_lshl_b32 s30, s30, 1\n"
    "s_and_b32 s31, s22, 1\n"
    "s_add_u32 s30, s30, s31\n"
    "s_lshl_b32 s30, s30, 1\n"
    "s_bfe_u32 s31, s23, 0x10003\n"
    "s_add_u32 s30, s30, s31\n"
    "s_lshl_b32 s30, s30, 16\n"
    "s_and_b32 s31, s23, 7\n"
    "s_lshl_b32 s31, s31, 5\n"
    "global_load_dwordx2 v[32:33], v78, s[38:39]\n"
    "s_add_u32 s30, s30, s31\n"
    "s_add_u32 s30, s30, 0x10800\n"
    "v_mul_f32_e64 v7, v74, v75\n"
    "s_add_u32 s20, s18, s30\n"
    "v_fmamk_f32 v12, v75, 0x0, v7\n"
    "v_fmamk_f32 v58, v75, 0x3f800000, v7\n"
    "v_fmamk_f32 v65, v75, 0x40000000, v7\n"
    "v_fmamk_f32 v72, v75, 0x40400000, v7\n"
    "s_addc_u32 s21, s19, 0\n"
    "v_lshlrev_b32 v2, 1, v15\n"
    "v_mul_f32 v13, 0x3d800000, v12\n"
    "v_mul_f32 v59, 0x3d800000, v58\n"
    "v_mul_f32 v66, 0x3d800000, v65\n"
    "v_mul_f32 v73, 0x3d800000, v72\n"
    "s_lshl_b32 s74, s27, 8\n"
    "v_xor_b32 v2, v2, v0\n"
    "v_mul_f32 v51, 0x3b800000, v12\n"
    "v_cos_f32 v54, v13\n"
    "v_sin_f32 v55, v13\n"
    "v_mul_f32 v60, 0x3b800000, v58\n"
    "v_cos_f32 v61, v59\n"
    "v_sin_f32 v62, v59\n"
    "v_mul_f32 v67, 0x3b800000, v65\n"
    "v_cos_f32 v68, v66\n"
    "v_sin_f32 v69, v66\n"
    "v_mul_f32 v79, 0x3b800000, v72\n"
    "v_cos_f32 v80, v73\n"
    "v_sin_f32 v81, v73\n"
    "v_and_b32_e64 v88, 16, v4\n"
    "s_add_u32 s74, s74, 0x20000\n"
    "v_and_b32 v2, 7, v2\n"
    "v_cos_f32 v56, v51\n"
    "v_sin_f32 v57, v51\n"
    "v_cos_f32 v63, v60\n"
    "v_sin_f32 v64, v60\n"
    "v_cos_f32 v70, v67\n"
    "v_sin_f32 v71, v67\n"
    "v_cos_f32 v82, v79\n"
    "v_sin_f32 v83, v79\n"
    "v_lshrrev_b32 v86, 5, v0\n"
    "v_bfe_u32 v87, v0, 1, 4\n"
    "v_lshlrev_b32 v85, 8, v88\n"
    "s_waitcnt lgkmcnt(3)\n"
    "s_waitcnt lgkmcnt(2)\n"
    "s_waitcnt lgkmcnt(1)\n"
    "s_waitcnt lgkmcnt(0)\n"
    "v_lshlrev_b32 v109, 8, v88\n"
    "s_lshr_b32 s28, s27, 1\n"
    "v_lshlrev_b32 v2, 4, v2\n"
    "v_lshrrev_b32_e64 v6, 3, v0\n"
    "v_lshl_add_u32 v85, v87, 4, v85\n"
    "v_lshlrev_b32_e64 v110, 16, v112\n"
    "v_and_b32 v111, 0xffff0000, v112\n"
    "v_lshlrev_b32_e64 v114, 16, v116\n"
    "v_and_b32 v115, 0xffff0000, v116\n"
    "v_lshlrev_b32_e64 v118, 16, v120\n"
    "v_and_b32 v119, 0xffff0000, v120\n"
    "v_lshlrev_b32_e64 v122, 16, v124\n"
    "v_and_b32 v123, 0xffff0000, v124\n"
    "v_lshl_add_u32 v109, v87, 4, v109\n"
    "v_and_b32 v126, 3, v86\n"
    "s_lshl_b32 s28, s28, 8\n"
    "v_lshl_add_u32 v29, v6, 10, v2\n"
    "v_mul_f32 v54, 0x3e800000, v54\n"
    "v_mul_f32 v55, 0x3e800000, v55\n"
    "v_mul_f32 v61, 0x3e800000, v61\n"
    "v_mul_f32 v62, 0x3e800000, v62\n"
    "v_mul_f32 v68, 0x3e800000, v68\n"
    "v_mul_f32 v69, 0x3e800000, v69\n"
    "v_mul_f32 v80, 0x3e800000, v80\n"
    "v_mul_f32 v81, 0x3e800000, v81\n"
    "v_lshl_add_u32 v84, v86, 5, v87\n"
    "v_lshl_add_u32 v85, v24, 3, v85\n"
    "v_lshlrev_b32 v112, 16, v113\n"
    "v_lshlrev_b32 v116, 16, v117\n"
    "v_lshlrev_b32 v120, 16, v121\n"
    "v_lshlrev_b32 v124, 16, v125\n"
    "v_lshl_add_u32 v109, v126, 2, v109\n"
    "s_add_u32 s28, s28, 0x22000\n"
    "global_load_dwordx2 v[52:53], v78, s[40:41]\n"
    "s_mov_b32 s47, 0x20000\n"
    "v_and_b32 v50, 0x1fff, v29\n"
    "v_cvt_pk_f16_f32 v8, v54, v55\n"
    "v_cvt_pk_f16_f32 v34, -v55, v54\n"
    "v_cvt_pk_f16_f32 v38, v54, -v55\n"
    "v_cvt_pk_f16_f32 v42, v55, v54\n"
    "v_cvt_pk_f16_f32 v46, v56, v57\n"
    "v_cvt_pk_f16_f32 v9, v61, v62\n"
    "v_cvt_pk_f16_f32 v35, -v62, v61\n"
    "v_cvt_pk_f16_f32 v39, v61, -v62\n"
    "v_cvt_pk_f16_f32 v43, v62, v61\n"
    "v_cvt_pk_f16_f32 v47, v63, v64\n"
    "v_cvt_pk_f16_f32 v10, v68, v69\n"
    "v_cvt_pk_f16_f32 v36, -v69, v68\n"
    "v_cvt_pk_f16_f32 v40, v68, -v69\n"
    "v_cvt_pk_f16_f32 v44, v69, v68\n"
    "v_cvt_pk_f16_f32 v48, v70, v71\n"
    "v_cvt_pk_f16_f32 v11, v80, v81\n"
    "v_cvt_pk_f16_f32 v37, -v81, v80\n"
    "v_cvt_pk_f16_f32 v41, v80, -v81\n"
    "v_cvt_pk_f16_f32 v45, v81, v80\n"
    "v_cvt_pk_f16_f32 v49, v82, v83\n"
    "v_lshl_add_u32 v84, v84, 8, v88\n"
    "v_add_u32_e64 v85, s74, v85\n"
    "v_and_b32 v113, 0xffff0000, v113\n"
    "v_and_b32 v117, 0xffff0000, v117\n"
    "v_and_b32 v121, 0xffff0000, v121\n"
    "v_and_b32 v125, 0xffff0000, v125\n"
    "v_add_u32 v109, s28, v109\n"
    "s_sleep 6\n"
    "global_load_dwordx4 v[54:57], v84, s[20:21] offset:-2048 sc1\n"
    "global_load_dwordx4 v[58:61], v84, s[20:21] offset:2048 sc1\n"
    "s_lshl_b32 s28, s27, 4\n"
    "v_add_u32 v89, s28, v14\n"
    "v_cvt_f32_u32 v89, v89\n"
    "v_mul_f32 v2, v74, v89\n"
    "v_mul_f32 v2, 0x3c800000, v2\n"
    "v_fmamk_f32 v12, v89, 0x0, v2\n"
    "v_fmamk_f32 v70, v89, 0x3d000000, v2\n"
    "v_fmamk_f32 v73, v89, 0x3c800000, v2\n"
    "v_fmamk_f32 v80, v89, 0x3d400000, v2\n"
    "v_fmamk_f32 v83, v89, 0x3e800000, v2\n"
    "v_fmamk_f32 v88, v89, 0x3e900000, v2\n"
    "v_fmamk_f32 v108, v89, 0x3e880000, v2\n"
    "v_fmamk_f32 v128, v89, 0x3e980000, v2\n"
    "v_cos_f32 v13, v12\n"
    "v_sin_f32 v51, v12\n"
    "v_cos_f32 v71, v70\n"
    "v_sin_f32 v72, v70\n"
    "v_cos_f32 v78, v73\n"
    "v_sin_f32 v79, v73\n"
    "v_cos_f32 v81, v80\n"
    "v_sin_f32 v82, v80\n"
    "v_cos_f32 v86, v83\n"
    "v_sin_f32 v87, v83\n"
    "v_cos_f32 v106, v88\n"
    "v_sin_f32 v107, v88\n"
    "v_cos_f32 v126, v108\n"
    "v_sin_f32 v127, v108\n"
    "v_cos_f32 v129, v128\n"
    "v_sin_f32 v130, v128\n"
    "v_mul_f32 v13, 0x3e800000, v13\n"
    "v_mul_f32 v51, 0x3e800000, v51\n"
    "v_mul_f32 v71, 0x3e800000, v71\n"
    "v_mul_f32 v72, 0x3e800000, v72\n"
    "v_mul_f32 v78, 0x3e800000, v78\n"
    "v_mul_f32 v79, 0x3e800000, v79\n"
    "v_mul_f32 v81, 0x3e800000, v81\n"
    "v_mul_f32 v82, 0x3e800000, v82\n"
    "v_mul_f32 v86, 0x3e800000, v86\n"
    "v_mul_f32 v87, 0x3e800000, v87\n"
    "v_mul_f32 v106, 0x3e800000, v106\n"
    "v_mul_f32 v107, 0x3e800000, v107\n"
    "v_mul_f32 v126, 0x3e800000, v126\n"
    "v_mul_f32 v127, 0x3e800000, v127\n"
    "v_mul_f32 v129, 0x3e800000, v129\n"
    "v_mul_f32 v130, 0x3e800000, v130\n"
    "v_cvt_pk_f16_f32 v90, v13, -v51\n"
    "v_cvt_pk_f16_f32 v98, v51, v13\n"
    "v_cvt_pk_f16_f32 v91, v71, -v72\n"
    "v_cvt_pk_f16_f32 v99, v72, v71\n"
    "v_cvt_pk_f16_f32 v92, v78, -v79\n"
    "v_cvt_pk_f16_f32 v100, v79, v78\n"
    "v_cvt_pk_f16_f32 v93, v81, -v82\n"
    "v_cvt_pk_f16_f32 v101, v82, v81\n"
    "v_cvt_pk_f16_f32 v94, v86, -v87\n"
    "v_cvt_pk_f16_f32 v102, v87, v86\n"
    "v_cvt_pk_f16_f32 v95, v106, -v107\n"
    "v_cvt_pk_f16_f32 v103, v107, v106\n"
    "v_cvt_pk_f16_f32 v96, v126, -v127\n"
    "v_cvt_pk_f16_f32 v104, v127, v126\n"
    "v_cvt_pk_f16_f32 v97, v129, -v130\n"
    "v_cvt_pk_f16_f32 v105, v130, v129\n"
    "s_waitcnt vmcnt(5)\n"
    "ds_write2st64_b32 v109, v26, v27 offset0:0 offset1:8\n"
    "s_waitcnt vmcnt(4)\n"
    "ds_write2st64_b32 v109, v30, v31 offset0:4 offset1:12\n"
    "s_waitcnt vmcnt(3)\n"
    "ds_write2st64_b32 v109, v32, v33 offset0:2 offset1:10\n"
    "s_waitcnt vmcnt(2)\n"
    "ds_write2st64_b32 v109, v52, v53 offset0:6 offset1:14\n"
    "s_waitcnt lgkmcnt(0)\n"
    "s_barrier\n"
    "s_lshl_b32 s28, s27, 11\n"
    "s_add_u32 s28, s28, 0x22000\n"
    "v_add_u32 v2, s28, v16\n"
    "s_lshl2_add_u32 s29, s23, s27\n"
    "ds_read_b128 v[70:73], v2 offset:0\n"
    "ds_read_b128 v[106:109], v2 offset:1024\n"
    "global_store_dwordx4 v4, v[140:143], s[52:53] offset:-4096 sc1\n"
    "s_cmp_eq_u32 s29, 0x7f\n"
    "s_cselect_b32 s38, 0.5, 1.0\n"
    "s_lshl3_add_u32 s43, s64, 0x100000\n"
    "s_add_u32 m0, s64, 0x8000\n"
    "v_bfe_u32 v12, v0, 1, 1\n"
    "v_mul_u32_u24 v12, 0x3f0, v12\n"
    "v_add_u32 v12, 32, v12\n"
    "v_lshrrev_b32 v26, 2, v5\n"
    "v_lshl_add_u32 v26, v26, 6, v170\n"
    "s_mov_b32 s51, 0x20000\n"
    "buffer_load_dwordx4 v50, s[44:47], s43 offen lds\n"
    "s_lshl3_add_u32 s43, s64, 0x108000\n"
    "s_add_u32 m0, s64, 0x9000\n"
    "s_cmp_ge_u32 s27, 2\n"
    "s_cselect_b32 s56, -1, 0\n"
    "v_and_b32 v12, s56, v12\n"
    "s_mul_i32 s56, s24, 0xc00\n"
    "buffer_load_dwordx4 v50, s[44:47], s43 offen lds\n"
    "s_cselect_b32 s43, 0, s37\n"
    "s_add_u32 s43, s43, 0x24000\n"
    "v_add3_u32 v26, v26, v12, s43\n"
    "s_mul_i32 s43, s25, 0x2c000\n"
    "ds_read_b64_tr_b16 v[134:135], v26 offset:0\n"
    "ds_read_b64_tr_b16 v[136:137], v26 offset:2048\n"
    "s_add_u32 s43, s43, 0x1210000\n"
    "s_add_u32 s48, s18, s43\n"
    "s_addc_u32 s49, s19, 0\n"
    "s_and_b32 s49, s49, 0xffff\n"
    "s_add_u32 s56, s56, 0\n"
    "s_add_u32 s50, s56, 0xc00\n"
    "s_lshl_b32 s28, s27, 11\n"
    "s_add_u32 s28, s28, 0x20000\n"
    "s_lshr_b32 s29, s27, 1\n"
    "s_and_b32 s30, s27, 1\n"
    "s_mul_i32 s29, s29, 0x1080\n"
    "s_lshl_b32 s30, s30, 3\n"
    "s_add_u32 s29, s29, s30\n"
    "s_add_u32 s29, s29, 0x25800\n"
    "s_lshl_b32 s31, s22, 5\n"
    "s_add_u32 s31, s31, s23\n"
    "s_lshl_b32 s31, s31, 8\n"
    "s_add_u32 s31, s31, 0x210000\n"
    "s_waitcnt lgkmcnt(0)\n"
    "buffer_store_dwordx4 v[134:137], v4, s[48:51], s56 offen sc1\n"
    "v_and_b32 v86, 1, v0\n"
    "s_add_u32 s40, s18, s31\n"
    "v_lshlrev_b32 v30, 16, v70\n"
    "v_lshlrev_b32 v31, 16, v71\n"
    "v_lshlrev_b32 v32, 16, v72\n"
    "v_lshlrev_b32 v33, 16, v73\n"
    "v_lshlrev_b32 v78, 16, v106\n"
    "v_lshlrev_b32 v79, 16, v107\n"
    "v_lshlrev_b32 v80, 16, v108\n"
    "v_lshlrev_b32 v81, 16, v109\n"
    "v_lshlrev_b32_e64 v51, 10, v15\n"
    "v_mul_u32_u24 v82, 0x1080, v86\n"
    "v_lshrrev_b32_e64 v83, 1, v0\n"
    "v_lshrrev_b32 v53, 4, v0\n"
    "s_addc_u32 s41, s19, 0\n"
    "v_and_b32 v70, 0xffff0000, v70\n"
    "v_and_b32 v71, 0xffff0000, v71\n"
    "v_and_b32 v72, 0xffff0000, v72\n"
    "v_and_b32 v73, 0xffff0000, v73\n"
    "v_and_b32 v106, 0xffff0000, v106\n"
    "v_and_b32 v107, 0xffff0000, v107\n"
    "v_and_b32 v108, 0xffff0000, v108\n"
    "v_and_b32 v109, 0xffff0000, v109\n"
    "v_lshl_add_u32 v51, v14, 4, v51\n"
    "v_lshl_add_u32 v52, v83, 4, v82\n"
    "v_lshlrev_b32_e64 v53, 12, v53\n"
    "s_add_u32 s80, s40, 0x100000\n"
    "v_mul_f32 v70, s38, v70\n"
    "v_mul_f32 v71, s38, v71\n"
    "v_mul_f32 v72, s38, v72\n"
    "v_mul_f32 v73, s38, v73\n"
    "v_mul_f32 v106, s38, v106\n"
    "v_mul_f32 v107, s38, v107\n"
    "v_mul_f32 v108, s38, v108\n"
    "v_mul_f32 v109, s38, v109\n"
    "s_mov_b32 s50, 0x2c000\n"
    "global_store_dwordx4 v4, v[140:143], s[52:53] offset:0 sc1\n"
    "v_add_u32 v27, s28, v16\n"
    "v_add_u32 v51, s29, v51\n"
    "v_add_u32 v52, 0x25800, v52\n"
    "v_add_lshl_u32 v53, v53, v14, 4\n"
    "s_addc_u32 s81, s41, 0\n"
    "s_waitcnt vmcnt(5)\n"
    "v_xor_b32 v62, s73, v54\n"
    "v_xor_b32 v63, s73, v58\n"
    "v_xor_b32 v64, s76, v55\n"
    "v_xor_b32 v65, s76, v59\n"
    "v_xor_b32 v66, s77, v56\n"
    "v_xor_b32 v67, s77, v60\n"
    "v_xor_b32 v68, s78, v57\n"
    "v_xor_b32 v69, s78, v61\n"
    "v_or3_b32 v6, v62, v63, v64\n"
    "v_or3_b32 v7, v65, v66, v67\n"
    "v_or3_b32 v6, v6, v68, v69\n"
    "v_or_b32 v6, v6, v7\n"
    "v_and_b32 v6, v77, v6\n"
    "v_cmp_ne_u32 vcc, 0, v6\n"
    "s_cbranch_vccnz .R1_%=\n"
    ".B2_%=:\n"
    "ds_write_b64 v85, v[62:63] offset:0\n"
    "ds_write_b64 v85, v[64:65] offset:1024\n"
    "ds_write_b64 v85, v[66:67] offset:2048\n"
    "ds_write_b64 v85, v[68:69] offset:3072\n"
    "s_waitcnt lgkmcnt(0)\n"
    "s_barrier\n"
    "ds_read_b128 v[160:163], v27 offset:1024\n"
    "ds_read_b128 v[134:137], v27 offset:0\n"
    "s_lshl3_add_u32 s43, s64, 0x110000\n"
    "s_add_u32 m0, s64, 0xa000\n"
    "v_min_u32_e64 v2, 7, v5\n"
    "buffer_load_dwordx4 v50, s[44:47], s43 offen lds\n"
    "s_lshl3_add_u32 s43, s65, 0x100000\n"
    "s_add_u32 m0, s65, 0x8000\n"
    "s_nop 0\n"
    "buffer_load_dwordx4 v50, s[44:47], s43 offen lds\n"
    "s_lshl2_add_u32 s43, s2, s27\n"
    "s_mul_i32 s43, s43, 8\n"
    "s_add_u32 s43, s43, 0\n"
    "s_min_u32 s43, s43, 0x1ff8\n"
    "s_waitcnt lgkmcnt(1)\n"
    "v_mfma_f32_16x16x32_f16 v[172:175], v[160:163], v[34:37], 0\n"
    "v_add_lshl_u32 v2, v2, s43, 7\n"
    "s_waitcnt lgkmcnt(0)\n"
    "v_mfma_f32_16x16x32_f16 v[164:167], v[160:163], v[8:11], 0\n"
    "s_lshl3_add_u32 s43, s65, 0x108000\n"
    "s_add_u32 m0, s65, 0x9000\n"
    "global_load_dword v17, v2, s[14:15]\n"
    "v_mfma_f32_16x16x32_f16 v[148:151], v[134:137], v[34:37], 0\n"
    "buffer_load_dwordx4 v50, s[44:47], s43 offen lds\n"
    "s_lshl3_add_u32 s43, s65, 0x110000\n"
    "v_mfma_f32_16x16x32_f16 v[144:147], v[134:137], v[8:11], 0\n"
    "s_add_u32 m0, s65, 0xa000\n"
    "v_cvt_pk_f16_f32 v175, v167, v175\n"
    "v_cvt_pk_f16_f32 v173, v165, v173\n"
    "v_pk_mul_f16 v167, v175, v49 op_sel:[0,0] op_sel_hi:[1,0]\n"
    "v_cvt_pk_f16_f32 v174, v166, v174\n"
    "v_pk_mul_f16 v165, v173, v47 op_sel:[0,0] op_sel_hi:[1,0]\n"
    "v_cvt_pk_f16_f32 v172, v164, v172\n"
    "v_pk_mul_f16 v166, v174, v48 op_sel:[0,0] op_sel_hi:[1,0]\n"
    "v_cvt_pk_f16_f32 v149, v145, v149\n"
    "v_pk_mul_f16 v164, v172, v46 op_sel:[0,0] op_sel_hi:[1,0]\n"
    "v_pk_fma_f16 v173, v173, v47, v165 op_sel:[1,1,0] op_sel_hi:[0,1,1] neg_hi:[0,1,0]\n"
    "v_pk_fma_f16 v174, v174, v48, v166 op_sel:[1,1,0] op_sel_hi:[0,1,1] neg_hi:[0,1,0]\n"
    "v_pk_fma_f16 v175, v175, v49, v167 op_sel:[1,1,0] op_sel_hi:[0,1,1] neg_hi:[0,1,0]\n"
    "v_cvt_pk_f16_f32 v148, v144, v148\n"
    "v_pk_fma_f16 v172, v172, v46, v164 op_sel:[1,1,0] op_sel_hi:[0,1,1] neg_hi:[0,1,0]\n"
    "v_cvt_pk_f16_f32 v151, v147, v151\n"
    "v_pk_mul_f16 v145, v149, v47 op_sel:[0,0] op_sel_hi:[1,0]\n"
    "v_mfma_f32_16x16x32_f16 v[176:179], v[8:11], v[172:175], 0\n"
    "v_pk_mul_f16 v144, v148, v46 op_sel:[0,0] op_sel_hi:[1,0]\n"
    "v_pk_mul_f16 v147, v151, v49 op_sel:[0,0] op_sel_hi:[1,0]\n"
    "v_mfma_f32_16x16x32_f16 v[180:183], v[34:37], v[172:175], 0\n"
    "v_cvt_pk_f16_f32 v150, v146, v150\n"
    "v_pk_fma_f16 v149, v149, v47, v145 op_sel:[1,1,0] op_sel_hi:[0,1,1] neg_hi:[0,1,0]\n"
    "v_pk_mul_f16 v146, v150, v48 op_sel:[0,0] op_sel_hi:[1,0]\n"
    "v_pk_fma_f16 v148, v148, v46, v144 op_sel:[1,1,0] op_sel_hi:[0,1,1] neg_hi:[0,1,0]\n"
    "v_pk_fma_f16 v151, v151, v49, v147 op_sel:[1,1,0] op_sel_hi:[0,1,1] neg_hi:[0,1,0]\n"
    "v_pk_fma_f16 v150, v150, v48, v146 op_sel:[1,1,0] op_sel_hi:[0,1,1] neg_hi:[0,1,0]\n"
    "v_mul_f32 v165, v177, v71\n"
    "v_mul_f32 v174, v178, v108\n"
    "v_mfma_f32_16x16x32_f16 v[152:155], v[8:11], v[148:151], 0\n"
    "v_mul_f32 v173, v177, v107\n"
    "v_mul_f32 v172, v176, v106\n"
    "v_mfma_f32_16x16x32_f16 v[156:159], v[34:37], v[148:151], 0\n"
    "v_mul_f32_e64 v164, v176, v70\n"
    "v_fma_f32 v165, -v181, v107, v165\n"
    "v_mul_f32 v167, v179, v73\n"
    "v_fmac_f32 v173, v181, v71\n"
    "v_mul_f32 v175, v179, v109\n"
    "v_fmac_f32 v174, v182, v72\n"
    "v_mul_f32_e64 v166, v178, v72\n"
    "v_fma_f32 v164, -v180, v106, v164\n"
    "v_fma_f32 v167, -v183, v109, v167\n"
    "v_fmac_f32 v172, v180, v70\n"
    "v_mul_f32 v150, v154, v80\n"
    "v_fma_f32 v166, -v182, v108, v166\n"
    "v_mul_f32 v144, v152, v30\n"
    "v_mul_f32 v149, v153, v79\n"
    "v_mul_f32 v146, v154, v32\n"
    "v_mul_f32 v148, v152, v78\n"
    "v_cvt_pk_f16_f32 v160, v164, v172\n"
    "v_cvt_pk_f16_f32 v161, v165, v173\n"
    "v_fmac_f32 v175, v183, v73\n"
    "v_mul_f32 v145, v153, v31\n"
    "v_fma_f32 v146, -v158, v80, v146\n"
    "v_cvt_pk_f16_f32 v162, v166, v174\n"
    "v_fmac_f32 v150, v158, v32\n"
    "v_mul_f32 v151, v155, v81\n"
    "v_mul_f32_e64 v147, v155, v33\n"
    "v_fmac_f32 v149, v157, v31\n"
    "v_fma_f32 v144, -v156, v78, v144\n"
    "v_fmac_f32 v148, v156, v30\n"
    "v_fma_f32 v145, -v157, v79, v145\n"
    "v_cvt_pk_f16_f32 v163, v167, v175\n"
    "v_fmac_f32 v151, v159, v33\n"
    "v_cvt_pk_f16_f32 v135, v145, v149\n"
    "v_mfma_f32_16x16x32_f16 v[172:175], v[160:163], v[42:45], 0\n"
    "v_fma_f32 v147, -v159, v81, v147\n"
    "v_cvt_pk_f16_f32 v136, v146, v150\n"
    "v_mfma_f32_16x16x32_f16 v[164:167], v[160:163], v[38:41], 0\n"
    "v_cvt_pk_f16_f32 v134, v144, v148\n"
    "v_cvt_pk_f16_f32 v137, v147, v151\n"
    "s_nop 1\n"
    "v_mfma_f32_16x16x32_f16 v[144:147], v[134:137], v[38:41], 0\n"
    "buffer_load_dwordx4 v50, s[44:47], s43 offen lds\n"
    "s_nop 1\n"
    "v_cvt_pk_f16_f32 v175, v167, v175\n"
    "v_mfma_f32_16x16x32_f16 v[148:151], v[134:137], v[42:45], 0\n"
    "v_cvt_pk_f16_f32 v172, v164, v172\n"
    "v_cvt_pk_f16_f32 v173, v165, v173\n"
    "v_pk_mul_f16 v167, v175, v49 op_sel:[0,0] op_sel_hi:[1,0]\n"
    "v_cvt_pk_f16_f32 v174, v166, v174\n"
    "v_pk_mul_f16 v165, v173, v47 op_sel:[0,0] op_sel_hi:[1,0]\n"
    "v_pk_mul_f16 v164, v172, v46 op_sel:[0,0] op_sel_hi:[1,0]\n"
    "v_pk_mul_f16 v166, v174, v48 op_sel:[0,0] op_sel_hi:[1,0]\n"
    "s_nop 0\n"
    "v_cvt_pk_f16_f32 v151, v147, v151\n"
    "v_cvt_pk_f16_f32 v148, v144, v148\n"
    "v_cvt_pk_f16_f32 v150, v146, v150\n"
    "v_cvt_pk_f16_f32 v149, v145, v149\n"
    "v_pk_fma_f16 v175, v175, v49, v167 op_sel:[1,1,0] op_sel_hi:[0,1,1] neg_lo:[0,1,0]\n"
    "v_pk_mul_f16 v147, v151, v49 op_sel:[0,0] op_sel_hi:[1,0]\n"
    "v_pk_fma_f16 v173, v173, v47, v165 op_sel:[1,1,0] op_sel_hi:[0,1,1] neg_lo:[0,1,0]\n"
    "v_pk_mul_f16 v145, v149, v47 op_sel:[0,0] op_sel_hi:[1,0]\n"
    "v_pk_mul_f16 v146, v150, v48 op_sel:[0,0] op_sel_hi:[1,0]\n"
    "v_pk_fma_f16 v149, v149, v47, v145 op_sel:[1,1,0] op_sel_hi:[0,1,1] neg_lo:[0,1,0]\n"
    "v_pk_fma_f16 v174, v174, v48, v166 op_sel:[1,1,0] op_sel_hi:[0,1,1] neg_lo:[0,1,0]\n"
    "v_pk_mul_f16 v144, v148, v46 op_sel:[0,0] op_sel_hi:[1,0]\n"
    "v_pk_fma_f16 v172, v172, v46, v164 op_sel:[1,1,0] op_sel_hi:[0,1,1] neg_lo:[0,1,0]\n"
    "v_pk_fma_f16 v148, v148, v46, v144 op_sel:[1,1,0] op_sel_hi:[0,1,1] neg_lo:[0,1,0]\n"
    "v_pk_fma_f16 v151, v151, v49, v147 op_sel:[1,1,0] op_sel_hi:[0,1,1] neg_lo:[0,1,0]\n"
    "v_mfma_f32_16x16x32_f16 v[176:179], v[38:41], v[172:175], 0\n"
    "v_pk_fma_f16 v150, v150, v48, v146 op_sel:[1,1,0] op_sel_hi:[0,1,1] neg_lo:[0,1,0]\n"
    "v_mfma_f32_16x16x32_f16 v[180:183], v[42:45], v[172:175], 0\n"
    "s_nop 0\n"
    "v_mfma_f32_16x16x32_f16 v[152:155], v[38:41], v[148:151], 0\n"
    "v_mfma_f32_16x16x32_f16 v[156:159], v[42:45], v[148:151], 0\n"
    "s_nop 4\n"
    "v_cvt_pk_f16_f32 v133, v179, v183\n"
    "v_cvt_pk_f16_f32 v127, v176, v180\n"
    "v_cvt_pk_f16_f32 v131, v178, v182\n"
    "v_cvt_pk_f16_f32 v129, v177, v181\n"
    "v_cvt_pk_f16_f32 v132, v155, v159\n"
    "v_cvt_pk_f16_f32 v130, v154, v158\n"
    "v_cvt_pk_f16_f32 v126, v152, v156\n"
    "v_cvt_pk_f16_f32 v128, v153, v157\n"
    "ds_write_b64 v51, v[132:133] offset:768\n"
    "ds_write_b64 v51, v[126:127] offset:0\n"
    "ds_write_b64 v51, v[130:131] offset:512\n"
    "ds_write_b64 v51, v[128:129] offset:256\n"
    "s_waitcnt vmcnt(5)\n"
    "s_waitcnt lgkmcnt(0)\n"
    "s_barrier\n"
    "ds_read_b128 v[54:57], v52\n"
    "ds_read_b128 v[58:61], v52 offset:2048\n"
    "s_waitcnt lgkmcnt(1)\n"
    "v_add_u32 v54, 0x20002, v54\n"
    "v_add_u32 v55, 0x20002, v55\n"
    "v_add_u32 v56, 0x20002, v56\n"
    "v_add_u32 v57, 0x20002, v57\n"
    "v_bfi_b32 v54, v77, s73, v54\n"
    "v_bfi_b32 v55, v77, s76, v55\n"
    "v_bfi_b32 v56, v77, s77, v56\n"
    "v_bfi_b32 v57, v77, s78, v57\n"
    "s_waitcnt lgkmcnt(0)\n"
    "global_store_dwordx4 v53, v[54:57], s[40:41] sc1\n"
    "v_add_u32 v58, 0x20002, v58\n"
    "v_add_u32 v59, 0x20002, v59\n"
    "v_add_u32 v60, 0x20002, v60\n"
    "v_add_u32 v61, 0x20002, v61\n"
    "v_bfi_b32 v58, v77, s73, v58\n"
    "v_bfi_b32 v59, v77, s76, v59\n"
    "v_bfi_b32 v60, v77, s77, v60\n"
    "v_bfi_b32 v61, v77, s78, v61\n"
    "global_store_dwordx4 v53, v[58:61], s[80:81] sc1\n"
    "s_lshl3_add_u32 s43, s66, 0x100000\n"
    "s_add_u32 m0, s66, 0x8000\n"
    "v_lshrrev_b32 v161, 2, v5\n"
    "v_bfe_u32 v163, v0, 3, 2\n"
    "v_lshl_add_u32 v162, v161, 7, v170\n"
    "v_and_b32 v12, v23, v24\n"
    "v_cvt_f32_u32 v13, v25\n"
    "v_add_f32_e64 v2, -1.0, v28\n"
    "v_lshl_add_u32 v162, v163, 5, v162\n"
    "v_fma_f32 v13, v13, 4.0, 1.0\n"
    "v_cvt_f32_u32 v12, v12\n"
    "v_fmac_f32 v12, v2, v13\n"
    "v_mul_f32 v12, 0x3e000000, v12\n"
    "v_fmamk_f32 v26, v2, 0x0, v12\n"
    "v_fmamk_f32 v41, v2, 0x3ec00000, v12\n"
    "v_fmamk_f32 v35, v2, 0x3e000000, v12\n"
    "v_fmamk_f32 v38, v2, 0x3e800000, v12\n"
    "v_sin_f32 v34, v26\n"
    "v_cos_f32 v36, v35\n"
    "v_sin_f32 v43, v41\n"
    "v_cos_f32 v27, v26\n"
    "v_sin_f32 v37, v35\n"
    "v_cos_f32 v42, v41\n"
    "v_sin_f32_e64 v40, v38\n"
    "v_mul_f32 v34, 0x3e000000, v34\n"
    "v_mul_f32 v37, 0x3e000000, v37\n"
    "v_cos_f32_e64 v39, v38\n"
    "v_mul_f32 v36, 0x3e000000, v36\n"
    "v_and_b32_e64 v165, 1, v161\n"
    "v_mul_f32 v27, 0x3e000000, v27\n"
    "v_mul_f32 v43, 0x3e000000, v43\n"
    "v_mul_f32 v42, 0x3e000000, v42\n"
    "v_mul_f32 v40, 0x3e000000, v40\n"
    "s_mov_b32 s39, 0xffffffff\n"
    "buffer_load_dwordx4 v50, s[44:47], s43 offen lds\n"
    "s_lshl3_add_u32 s43, s66, 0x108000\n"
    "s_add_u32 m0, s66, 0x9000\n"
    "v_cvt_pk_f16_f32 v8, v27, -v34\n"
    "v_mul_f32 v39, 0x3e000000, v39\n"
    "v_cvt_pk_f16_f32 v30, -v34, -v27\n"
    "v_lshl_add_u32 v163, v25, 1, v24\n"
    "s_mov_b32 s38, 0\n"
    "buffer_load_dwordx4 v50, s[44:47], s43 offen lds\n"
    "s_lshl3_add_u32 s43, s66, 0x110000\n"
    "s_add_u32 m0, s66, 0xa000\n"
    "v_cvt_pk_f16_f32 v31, -v37, -v36\n"
    "v_cvt_pk_f16_f32 v33, -v43, -v42\n"
    "v_cvt_pk_f16_f32 v11, v42, -v43\n"
    "v_cvt_pk_f16_f32 v32, -v40, -v39\n"
    "v_lshl_add_u32 v165, v165, 7, v170\n"
    "v_cvt_pk_f16_f32 v9, v36, -v37\n"
    "v_cvt_pk_f16_f32 v10, v39, -v40\n"
    "v_cndmask_b32_e64 v49, v11, v33, s[38:39]\n"
    "v_cndmask_b32_e64 v46, v8, v30, s[38:39]\n"
    "v_cndmask_b32_e64 v48, v10, v32, s[38:39]\n"
    "v_mul_u32_u24 v164, 0x120, v163\n"
    "v_cndmask_b32_e64 v47, v9, v31, s[38:39]\n"
    "s_mov_b32 s38, 0xff00ff00\n"
    "buffer_load_dwordx4 v50, s[44:47], s43 offen lds\n"
    "s_lshl3_add_u32 s43, s67, 0x100000\n"
    "s_add_u32 m0, s67, 0x8000\n"
    "s_mov_b32 s39, 0xff00ff\n"
    "v_cndmask_b32_e64 v80, v10, v32, s[38:39]\n"
    "v_cndmask_b32_e64 v81, v11, v33, s[38:39]\n"
    "v_cndmask_b32_e64 v78, v8, v30, s[38:39]\n"
    "v_cndmask_b32_e64 v79, v9, v31, s[38:39]\n"
    "v_mov_b32 v45, 0x80008000\n"
    "s_mov_b32 s38, 0xff00ff00\n"
    "buffer_load_dwordx4 v50, s[44:47], s43 offen lds\n"
    "s_lshl3_add_u32 s43, s67, 0x108000\n"
    "s_add_u32 m0, s67, 0x9000\n"
    "s_mov_b32 s39, 0xff00ff00\n"
    "s_mov_b32 s40, 0xff00ff00\n"
    "v_lshlrev_b32_e64 v181, 7, v24\n"
    "v_cndmask_b32_e64 v52, 0, v45, s[38:39]\n"
    "s_mov_b32 s41, 0xff00ff00\n"
    "buffer_load_dwordx4 v50, s[44:47], s43 offen lds\n"
    "s_lshl3_add_u32 s43, s67, 0x110000\n"
    "s_add_u32 m0, s67, 0xa000\n"
    "v_bfe_u32 v167, v0, 1, 2\n"
    "v_cndmask_b32_e64 v72, v10, v32, s[40:41]\n"
    "s_mov_b32 s38, 0xff00ff00\n"
    "buffer_load_dwordx4 v50, s[44:47], s43 offen lds\n"
    "s_xor_b32 s43, s59, 0x68000\n"
    "s_add_u32 m0, s37, 0x14000\n"
    "v_cndmask_b32_e64 v71, v9, v31, s[40:41]\n"
    "v_mul_u32_u24 v85, 0x110, v167\n"
    "v_cndmask_b32_e64 v73, v11, v33, s[40:41]\n"
    "s_mov_b32 s39, 0xff00ff00\n"
    "buffer_load_dwordx4 v29, s[44:47], s43 offen lds\n"
    "s_xor_b32 s43, s59, 0xe8000\n"
    "s_add_u32 m0, s37, 0x15000\n"
    "v_lshl_add_u32 v181, v25, 3, v181\n"
    "v_cndmask_b32_e64 v70, v8, v30, s[40:41]\n"
    "v_lshl_add_u32 v85, v25, 10, v85\n"
    "s_mov_b32 s41, 0xff00ff\n"
    "buffer_load_dwordx4 v29, s[44:47], s43 offen lds\n"
    "s_xor_b32 s43, s59, 0x78000\n"
    "s_add_u32 m0, s37, 0x16000\n"
    "s_mov_b32 s40, 0xffffffff\n"
    "v_and_b32 v182, 7, v0\n"
    "v_cndmask_b32_e64 v51, 0, v45, s[40:41]\n"
    "v_lshl_add_u32 v85, v24, 7, v85\n"
    "v_and_b32_e64 v75, 16, v4\n"
    "v_lshrrev_b32 v168, 1, v75\n"
    "v_lshl_add_u32 v85, v23, 6, v85\n"
    "s_mov_b32 s41, 0xffffffff\n"
    "buffer_load_dwordx4 v29, s[44:47], s43 offen lds\n"
    "s_xor_b32 s43, s59, 0xf8000\n"
    "s_add_u32 m0, s37, 0x17000\n"
    "s_add_u32 s58, s69, 0\n"
    "s_mov_b32 s40, 0xff00ff\n"
    "v_xor_b32 v173, v14, v15\n"
    "v_lshrrev_b32_e64 v74, 1, v0\n"
    "v_cndmask_b32_e64 v53, 0, v45, s[40:41]\n"
    "v_lshlrev_b32_e64 v173, 4, v173\n"
    "s_mul_i32 s92, s34, 0x9e3779b1\n"
    "buffer_load_dwordx4 v29, s[44:47], s43 offen lds\n"
    "s_add_u32 m0, s37, 0\n"
    "v_lshl_add_u32 v173, v15, 8, v173\n"
    "v_xor_b32 v145, v79, v53\n"
    "v_lshl_add_u32 v84, v74, 9, v75\n"
    "v_xor_b32_e64 v108, v80, v51\n"
    "s_mov_b32 s86, 0x8000\n"
    "buffer_load_dwordx4 v76, s[60:63], s58 offen lds\n"
    "s_add_u32 m0, s37, 0x17fc0\n"
    "v_xor_b32 v127, v9, v52\n"
    "v_xor_b32 v132, v48, v52\n"
    "v_mov_b32 v189, 0\n"
    "v_xor_b32 v106, v78, v51\n"
    "v_xor_b32 v133, v49, v52\n"
    "v_xor_b32 v134, v70, v52\n"
    "v_mov_b32 v190, 0\n"
    "v_mov_b32 v188, 0\n"
    "v_xor_b32 v126, v8, v52\n"
    "v_xor_b32 v128, v10, v52\n"
    "v_xor_b32 v109, v81, v51\n"
    "v_xor_b32 v131, v47, v52\n"
    "s_mov_b32 s89, 0x3e60ec05\n"
    "buffer_load_dwordx4 v76, s[60:63], s58 offen offset:64 lds\n"
    "s_add_u32 s58, s69, 0x15800\n"
    "s_add_u32 m0, s37, 0x1000\n"
    "v_xor_b32_e64 v147, v81, v53\n"
    "v_mul_lo_u32 v44, v24, v45\n"
    "v_xor_b32_e64 v137, v73, v52\n"
    "s_mov_b32 s87, 0x20000\n"
    "buffer_load_dwordx4 v76, s[60:63], s58 offen lds\n"
    "s_add_u32 m0, s37, 0x18fc0\n"
    "v_mov_b32 v185, 0\n"
    "v_mov_b32 v186, 0\n"
    "v_mov_b32 v187, 0\n"
    "v_xor_b32 v135, v71, v52\n"
    "v_xor_b32 v129, v11, v52\n"
    "v_xor_b32 v144, v78, v53\n"
    "v_add_u32 v179, 0x27800, v16\n"
    "v_xor_b32 v130, v46, v52\n"
    "v_xor_b32 v107, v79, v51\n"
    "v_xor_b32 v136, v72, v52\n"
    "v_xor_b32 v146, v80, v53\n"
    "v_add_u32 v173, 0x20000, v173\n"
    "v_mov_b32 v184, 0\n"
    "v_mov_b32 v191, 0\n"
    "buffer_load_dwordx4 v76, s[60:63], s58 offen offset:64 lds\n"
    "s_add_u32 s58, s69, 0x2b000\n"
    "s_add_u32 m0, s37, 0x2000\n"
    "s_nop 0\n"
    "buffer_load_dwordx4 v76, s[60:63], s58 offen lds\n"
    "s_add_u32 m0, s37, 0x19fc0\n"
    "s_lshr_b32 s28, s26, 3\n"
    "s_lshl_b32 s28, s28, 16\n"
    "s_and_b32 s29, s26, 7\n"
    "s_lshl_b32 s29, s29, 5\n"
    "s_add_u32 s28, s28, s29\n"
    "s_add_u32 s28, s28, 0x210000\n"
    "s_add_u32 s20, s18, s28\n"
    "s_addc_u32 s21, s19, 0\n"
    "s_and_b32 s75, s27, 1\n"
    "s_lshr_b32 s79, s27, 1\n"
    "s_lshl_b32 s28, s75, 12\n"
    "s_add_u32 s28, s28, 0x10000\n"
    "s_add_u32 s80, s35, 0x10000\n"
    "s_lshl_b32 s30, s27, 11\n"
    "s_add_u32 s30, s30, 0x20000\n"
    "s_lshl_b32 s31, s27, 8\n"
    "s_add_u32 s31, s31, 0x25800\n"
    "s_lshl_b32 s74, s27, 5\n"
    "s_add_u32 s90, s52, 0x200000\n"
    "v_add_u32 v82, s28, v162\n"
    "s_addc_u32 s91, s53, 0\n"
    "s_lshl2_add_u32 s28, s24, s27\n"
    "s_lshl_b32 s28, s28, 15\n"
    "s_add_u32 s28, s28, 0x410000\n"
    "s_add_u32 s84, s18, s28\n"
    "s_addc_u32 s85, s19, 0\n"
    "s_and_b32 s85, s85, 0xffff\n"
    "s_add_u32 s28, s68, 0\n"
    "s_mul_i32 s29, s75, 0x6000\n"
    "v_add_u32 v165, s29, v165\n"
    "s_and_b32 s28, s28, 3\n"
    "s_lshl_b32 s81, s28, 11\n"
    "v_add_u32 v164, v164, v165\n"
    "s_add_u32 s28, s68, 1\n"
    "v_lshl_add_u32 v165, v15, 9, v165\n"
    "s_and_b32 s28, s28, 3\n"
    "s_lshl_b32 s82, s28, 11\n"
    "v_lshl_add_u32 v165, v25, 6, v165\n"
    "s_add_u32 s28, s68, 2\n"
    "v_add_u32 v166, 0, v165\n"
    "v_add_u32 v149, 0x1400, v164\n"
    "v_add_u32_e64 v181, s31, v181\n"
    "v_cndmask_b32_e64 v149, v149, v166, s[38:39]\n"
    "s_and_b32 s28, s28, 3\n"
    "v_lshl_add_u32 v181, v23, 4, v181\n"
    "s_lshl_b32 s83, s28, 11\n"
    "v_add_u32 v153, 0x2000, v164\n"
    "v_add_u32 v178, 0x2000, v181\n"
    "v_add_u32 v166, 0x120, v165\n"
    "v_lshl_add_u32 v181, v182, 10, v181\n"
    "s_add_u32 s28, s68, 3\n"
    "v_cndmask_b32_e64 v153, v153, v166, s[38:39]\n"
    "v_add3_u32 v85, v85, v168, s30\n"
    "v_lshl_add_u32 v174, v170, 2, v181\n"
    "s_and_b32 s28, s28, 3\n"
    "v_add_u32 v166, 0x800, v164\n"
    "v_add_u32 v157, 0x2c00, v164\n"
    "v_add_u32 v88, s29, v162\n"
    "v_xor_b32 v183, s74, v4\n"
    "v_xor_b32_e64 v172, 48, v85\n"
    "v_cndmask_b32_e64 v157, v157, v166, s[38:39]\n"
    "v_xor_b32_e64 v175, 32, v174\n"
    "v_xor_b32 v152, 0x60, v149\n"
    "v_xor_b32 v86, 64, v82\n"
    "s_lshl_b32 s88, s28, 11\n"
    "v_xor_b32 v177, 0x60, v174\n"
    "v_xor_b32 v176, 64, v174\n"
    "v_xor_b32 v154, 32, v153\n"
    "v_xor_b32_e64 v155, 64, v153\n"
    "v_xor_b32 v148, 0x60, v88\n"
    "v_xor_b32 v83, 32, v82\n"
    "v_xor_b32 v138, 32, v88\n"
    "s_add_u32 s92, s92, 0x7f4a7c15\n"
    "v_xor_b32_e64 v139, 64, v88\n"
    "v_xor_b32 v160, 0x60, v157\n"
    "v_xor_b32 v151, 64, v149\n"
    "v_xor_b32 v150, 32, v149\n"
    "v_add_u32 v180, 0x25800, v183\n"
    "v_xor_b32 v156, 0x60, v153\n"
    "v_xor_b32 v169, 16, v85\n"
    "s_sub_u32 s93, 0, s92\n"
    "buffer_load_dwordx4 v76, s[60:63], s58 offen offset:64 lds\n"
    "v_xor_b32 v158, 32, v157\n"
    "v_xor_b32 v171, 32, v85\n"
    "v_xor_b32 v87, 0x60, v82\n"
    "v_xor_b32_e64 v159, 64, v157\n"
    "global_load_dwordx4 v[54:57], v84, s[20:21] offset:0 sc1\n"
    "global_load_dwordx4 v[58:61], v84, s[20:21] offset:256 sc1\n"
    "s_waitcnt vmcnt(8)\n"
    "s_barrier\n"
    "ds_read_b64_tr_b16 a[0:1], v82 offset:16384\n"
    "ds_read_b64_tr_b16 a[2:3], v82 offset:18432\n"
    "ds_read_b64_tr_b16 a[4:5], v83 offset:16384\n"
    "ds_read_b64_tr_b16 a[6:7], v83 offset:18432\n"
    "ds_read_b64_tr_b16 a[8:9], v86 offset:16384\n"
    "ds_read_b64_tr_b16 a[10:11], v86 offset:18432\n"
    "ds_read_b64_tr_b16 a[12:13], v87 offset:16384\n"
    "ds_read_b64_tr_b16 a[14:15], v87 offset:18432\n"
    "s_add_u32 s58, s69, 0x40800\n"
    "s_add_u32 m0, s37, 0x3000\n"
    "s_nop 0\n"
    "buffer_load_dwordx4 v76, s[60:63], s58 offen lds\n"
    "s_add_u32 m0, s37, 0x1afc0\n"
    "v_mul_f32 v26, v13, v89\n"
    "v_mul_f32 v26, 0x3b000000, v26\n"
    "v_cvt_f32_u32_e64 v2, s26\n"
    "v_mul_f32 v2, 0x3a000000, v2\n"
    "v_cvt_f32_u32 v12, v24\n"
    "v_fmac_f32 v26, v2, v12\n"
    "ds_read_b64_tr_b16 a[16:17], v82 offset:24576\n"
    "ds_read_b64_tr_b16 a[18:19], v82 offset:26624\n"
    "ds_read_b64_tr_b16 a[20:21], v83 offset:24576\n"
    "ds_read_b64_tr_b16 a[22:23], v83 offset:26624\n"
    "ds_read_b64_tr_b16 a[24:25], v86 offset:24576\n"
    "ds_read_b64_tr_b16 a[26:27], v86 offset:26624\n"
    "ds_read_b64_tr_b16 a[28:29], v87 offset:24576\n"
    "ds_read_b64_tr_b16 a[30:31], v87 offset:26624\n"
    "v_fmamk_f32 v164, v2, 0x40000000, v26\n"
    "v_fmamk_f32 v27, v2, 0x0, v26\n"
    "v_fmamk_f32 v197, v2, 0x40800000, v26\n"
    "v_fmamk_f32 v210, v2, 0x40c00000, v26\n"
    "v_fmamk_f32 v207, v89, 0x3bc00000, v197\n"
    "v_fmamk_f32 v183, v89, 0x3b800000, v164\n"
    "v_fmamk_f32 v201, v89, 0x3b000000, v197\n"
    "v_fmamk_f32 v211, v89, 0x0, v210\n"
    "v_fmamk_f32 v78, v89, 0x3b800000, v27\n"
    "v_fmamk_f32 v28, v89, 0x0, v27\n"
    "v_fmamk_f32 v217, v89, 0x3b800000, v210\n"
    "ds_read_b64_tr_b16 a[32:33], v88 offset:35840\n"
    "ds_read_b64_tr_b16 a[34:35], v149 offset:32768\n"
    "ds_read_b64_tr_b16 a[36:37], v138 offset:35840\n"
    "ds_read_b64_tr_b16 a[38:39], v150 offset:32768\n"
    "ds_read_b64_tr_b16 a[40:41], v139 offset:35840\n"
    "ds_read_b64_tr_b16 a[42:43], v151 offset:32768\n"
    "ds_read_b64_tr_b16 a[44:45], v148 offset:35840\n"
    "ds_read_b64_tr_b16 a[46:47], v152 offset:32768\n"
    "v_fmamk_f32 v45, v89, 0x3b000000, v27\n"
    "v_fmamk_f32 v198, v89, 0x0, v197\n"
    "v_fmamk_f32 v204, v89, 0x3b800000, v197\n"
    "v_sin_f32 v209, v207\n"
    "v_cos_f32 v192, v183\n"
    "v_sin_f32 v193, v183\n"
    "v_cos_f32 v208, v207\n"
    "v_fmamk_f32 v194, v89, 0x3bc00000, v164\n"
    "v_sin_f32_e64 v75, v45\n"
    "v_fmamk_f32 v214, v89, 0x3b000000, v210\n"
    "ds_read_b64_tr_b16 a[48:49], v88 offset:38912\n"
    "ds_read_b64_tr_b16 a[50:51], v153 offset:32768\n"
    "ds_read_b64_tr_b16 a[52:53], v138 offset:38912\n"
    "ds_read_b64_tr_b16 a[54:55], v154 offset:32768\n"
    "ds_read_b64_tr_b16 a[56:57], v139 offset:38912\n"
    "ds_read_b64_tr_b16 a[58:59], v155 offset:32768\n"
    "ds_read_b64_tr_b16 a[60:61], v148 offset:38912\n"
    "ds_read_b64_tr_b16 a[62:63], v156 offset:32768\n"
    "v_fmamk_f32 v165, v89, 0x0, v164\n"
    "v_sin_f32_e64 v43, v28\n"
    "v_fmamk_f32 v168, v89, 0x3b000000, v164\n"
    "v_fmamk_f32 v220, v89, 0x3bc00000, v210\n"
    "v_sin_f32_e64 v206, v204\n"
    "v_fmamk_f32 v81, v89, 0x3bc00000, v27\n"
    "v_cos_f32 v79, v78\n"
    "v_sin_f32 v213, v211\n"
    "v_cos_f32 v199, v198\n"
    "v_cos_f32 v202, v201\n"
    "v_sin_f32_e64 v200, v198\n"
    "ds_read_b64_tr_b16 a[64:65], v88 offset:41984\n"
    "ds_read_b64_tr_b16 a[66:67], v157 offset:32768\n"
    "ds_read_b64_tr_b16 a[68:69], v138 offset:41984\n"
    "ds_read_b64_tr_b16 a[70:71], v158 offset:32768\n"
    "ds_read_b64_tr_b16 a[72:73], v139 offset:41984\n"
    "ds_read_b64_tr_b16 a[74:75], v159 offset:32768\n"
    "ds_read_b64_tr_b16 a[76:77], v148 offset:41984\n"
    "ds_read_b64_tr_b16 a[78:79], v160 offset:32768\n"
    "v_cos_f32 v166, v165\n"
    "v_sin_f32 v80, v78\n"
    "v_sin_f32 v216, v214\n"
    "v_sin_f32 v196, v194\n"
    "v_cos_f32 v205, v204\n"
    "v_cos_f32 v218, v217\n"
    "v_cos_f32 v74, v45\n"
    "v_sin_f32 v182, v168\n"
    "v_cos_f32 v215, v214\n"
    "v_cos_f32 v195, v194\n"
    "ds_read_b64_tr_b16 a[80:81], v88 offset:48128\n"
    "ds_read_b64_tr_b16 a[82:83], v149 offset:45056\n"
    "ds_read_b64_tr_b16 a[84:85], v138 offset:48128\n"
    "ds_read_b64_tr_b16 a[86:87], v150 offset:45056\n"
    "ds_read_b64_tr_b16 a[88:89], v139 offset:48128\n"
    "ds_read_b64_tr_b16 a[90:91], v151 offset:45056\n"
    "ds_read_b64_tr_b16 a[92:93], v148 offset:48128\n"
    "ds_read_b64_tr_b16 a[94:95], v152 offset:45056\n"
    "v_cos_f32 v42, v28\n"
    "v_sin_f32 v162, v81\n"
    "v_cos_f32 v181, v168\n"
    "v_cos_f32 v212, v211\n"
    "v_sin_f32 v167, v165\n"
    "v_cos_f32 v161, v81\n"
    "v_sin_f32 v219, v217\n"
    "v_cos_f32 v221, v220\n"
    "v_sin_f32 v203, v201\n"
    "v_sin_f32 v222, v220\n"
    "v_cvt_pk_f16_f32 v32, v79, v80\n"
    "ds_read_b64_tr_b16 a[96:97], v88 offset:51200\n"
    "ds_read_b64_tr_b16 a[98:99], v153 offset:45056\n"
    "ds_read_b64_tr_b16 a[100:101], v138 offset:51200\n"
    "ds_read_b64_tr_b16 a[102:103], v154 offset:45056\n"
    "ds_read_b64_tr_b16 a[104:105], v139 offset:51200\n"
    "ds_read_b64_tr_b16 a[106:107], v155 offset:45056\n"
    "ds_read_b64_tr_b16 a[108:109], v148 offset:51200\n"
    "ds_read_b64_tr_b16 a[110:111], v156 offset:45056\n"
    "v_cvt_pk_f16_f32 v41, v208, v209\n"
    "v_cvt_pk_f16_f32 v36, v192, v193\n"
    "v_cvt_pk_f16_f32 v38, v199, v200\n"
    "v_cvt_pk_f16_f32 v30, v42, v43\n"
    "v_cvt_pk_f16_f32 v31, v74, v75\n"
    "v_cvt_pk_f16_f32 v40, v205, v206\n"
    "v_cvt_pk_f16_f32 v35, v181, v182\n"
    "v_cvt_pk_f16_f32 v50, v212, v213\n"
    "v_cvt_pk_f16_f32 v39, v202, v203\n"
    "v_cvt_pk_f16_f32 v37, v195, v196\n"
    "ds_read_b64_tr_b16 a[112:113], v88 offset:54272\n"
    "ds_read_b64_tr_b16 a[114:115], v157 offset:45056\n"
    "ds_read_b64_tr_b16 a[116:117], v138 offset:54272\n"
    "ds_read_b64_tr_b16 a[118:119], v158 offset:45056\n"
    "ds_read_b64_tr_b16 a[120:121], v139 offset:54272\n"
    "ds_read_b64_tr_b16 a[122:123], v159 offset:45056\n"
    "ds_read_b64_tr_b16 a[124:125], v148 offset:54272\n"
    "ds_read_b64_tr_b16 a[126:127], v160 offset:45056\n"
    "buffer_load_dwordx4 v76, s[60:63], s58 offen offset:64 lds\n"
    "s_add_u32 s58, s70, 0\n"
    "s_add_u32 m0, s37, 0x4000\n"
    "v_cvt_pk_f16_f32 v34, v166, v167\n"
    "v_cvt_pk_f16_f32 v51, v215, v216\n"
    "v_cvt_pk_f16_f32 v53, v221, v222\n"
    "v_cvt_pk_f16_f32 v33, v161, v162\n"
    "v_cvt_pk_f16_f32 v52, v218, v219\n"
    "buffer_load_dwordx4 v76, s[60:63], s58 offen lds\n"
    "s_add_u32 m0, s37, 0x1bfc0\n"
    "s_nop 0\n"
    "buffer_load_dwordx4 v76, s[60:63], s58 offen offset:64 lds\n"
    "s_waitcnt vmcnt(4)\n"
    "v_xor_b32 v62, s73, v54\n"
    "v_xor_b32 v63, s73, v58\n"
    "v_xor_b32 v64, s76, v55\n"
    "v_xor_b32 v65, s76, v59\n"
    "v_xor_b32 v66, s77, v56\n"
    "v_xor_b32 v67, s77, v60\n"
    "v_xor_b32 v68, s78, v57\n"
    "v_xor_b32_e64 v69, s78, v61\n"
    "v_or3_b32 v6, v62, v63, v64\n"
    "v_or3_b32 v7, v65, v66, v67\n"
    "v_or3_b32 v6, v6, v68, v69\n"
    "v_or_b32 v6, v6, v7\n"
    "v_and_b32 v6, v77, v6\n"
    "v_cmp_ne_u32_e64 vcc, 0, v6\n"
    "s_cbranch_vccnz .R5_%=\n"
    ".B6_%=:\n"
    "ds_write_b64 v85, v[62:63]\n"
    "ds_write_b64 v169, v[64:65]\n"
    "ds_write_b64 v171, v[66:67]\n"
    "ds_write_b64 v172, v[68:69]\n"
    "s_waitcnt lgkmcnt(0)\n"
    "s_barrier\n"
    "ds_read_b128 v[78:81], v173 offset:0\n"
    "ds_read_b128 v[164:167], v173 offset:2048\n"
    "ds_read_b128 v[148:151], v173 offset:1024\n"
    "ds_read_b128 v[204:207], v173 offset:4096\n"
    "ds_read_b128 v[192:195], v173 offset:3072\n"
    "ds_read_b128 v[208:211], v173 offset:5120\n"
    "ds_read_b128 v[220:223], v173 offset:6144\n"
    "s_add_u32 s58, s70, 0x15800\n"
    "ds_read_b128 v[224:227], v173 offset:7168\n"
    "s_add_u32 m0, s37, 0x5000\n"
    "s_nop 0\n"
    "buffer_load_dwordx4 v76, s[60:63], s58 offen lds\n"
    "s_add_u32 m0, s37, 0x1cfc0\n"
    "s_waitcnt lgkmcnt(7)\n"
    "v_mfma_f32_16x16x32_f16 v[62:65], v[78:81], v[90:93], 0\n"
    "s_waitcnt lgkmcnt(6)\n"
    "buffer_load_dwordx4 v76, s[60:63], s58 offen offset:64 lds\n"
    "v_mfma_f32_16x16x32_f16 v[66:69], v[78:81], v[98:101], 0\n"
    "s_add_u32 s58, s70, 0x2b000\n"
    "s_add_u32 m0, s37, 0x6000\n"
    "s_waitcnt lgkmcnt(5)\n"
    "v_mfma_f32_16x16x32_f16 v[66:69], v[148:151], v[102:105], v[66:69]\n"
    "buffer_load_dwordx4 v76, s[60:63], s58 offen lds\n"
    "s_add_u32 m0, s37, 0x1dfc0\n"
    "v_mfma_f32_16x16x32_f16 v[62:65], v[148:151], v[94:97], v[62:65]\n"
    "s_waitcnt lgkmcnt(4)\n"
    "s_waitcnt lgkmcnt(3)\n"
    "v_mfma_f32_16x16x32_f16 v[152:155], v[164:167], v[90:93], 0\n"
    "s_waitcnt lgkmcnt(2)\n"
    "v_mfma_f32_16x16x32_f16 v[156:159], v[164:167], v[98:101], 0\n"
    "buffer_load_dwordx4 v76, s[60:63], s58 offen offset:64 lds\n"
    "s_add_u32 s58, s70, 0x40800\n"
    "v_mfma_f32_16x16x32_f16 v[196:199], v[204:207], v[90:93], 0\n"
    "s_add_u32 m0, s37, 0x7000\n"
    "s_waitcnt lgkmcnt(1)\n"
    "v_cvt_pk_f16_f32 v66, v62, v66\n"
    "v_mfma_f32_16x16x32_f16 v[200:203], v[204:207], v[98:101], 0\n"
    "s_waitcnt lgkmcnt(0)\n"
    "buffer_load_dwordx4 v76, s[60:63], s58 offen lds\n"
    "v_cvt_pk_f16_f32 v68, v64, v68\n"
    "v_mfma_f32_16x16x32_f16 v[156:159], v[192:195], v[102:105], v[156:159]\n"
    "s_add_u32 m0, s37, 0x1efc0\n"
    "v_cvt_pk_f16_f32 v67, v63, v67\n"
    "v_cvt_pk_f16_f32 v69, v65, v69\n"
    "v_mfma_f32_16x16x32_f16 v[152:155], v[192:195], v[94:97], v[152:155]\n"
    "v_pk_mul_f16 v62, v66, v30 op_sel:[0,0] op_sel_hi:[1,0]\n"
    "v_pk_mul_f16 v64, v68, v32 op_sel:[0,0] op_sel_hi:[1,0]\n"
    "v_mfma_f32_16x16x32_f16 v[216:219], v[220:223], v[98:101], 0\n"
    "v_pk_mul_f16 v63, v67, v31 op_sel:[0,0] op_sel_hi:[1,0]\n"
    "v_pk_mul_f16 v65, v69, v33 op_sel:[0,0] op_sel_hi:[1,0]\n"
    "v_mfma_f32_16x16x32_f16 v[196:199], v[208:211], v[94:97], v[196:199]\n"
    "v_pk_fma_f16 v66, v66, v30, v62 op_sel:[1,1,0] op_sel_hi:[0,1,1] neg_lo:[0,1,0]\n"
    "v_pk_fma_f16 v69, v69, v33, v65 op_sel:[1,1,0] op_sel_hi:[0,1,1] neg_lo:[0,1,0]\n"
    "v_mfma_f32_16x16x32_f16 v[200:203], v[208:211], v[102:105], v[200:203]\n"
    "v_pk_fma_f16 v67, v67, v31, v63 op_sel:[1,1,0] op_sel_hi:[0,1,1] neg_lo:[0,1,0]\n"
    "v_pk_fma_f16 v68, v68, v32, v64 op_sel:[1,1,0] op_sel_hi:[0,1,1] neg_lo:[0,1,0]\n"
    "v_mfma_f32_16x16x32_f16 v[212:215], v[220:223], v[90:93], 0\n"
    "v_cvt_pk_f16_f32 v158, v154, v158\n"
    "v_xor_b32_e64 v65, v69, v44\n"
    "v_mfma_f32_16x16x32_f16 v[216:219], v[224:227], v[102:105], v[216:219]\n"
    "v_cvt_pk_f16_f32 v157, v153, v157\n"
    "v_xor_b32_e64 v62, v66, v44\n"
    "v_mfma_f32_16x16x32_f16 v[212:215], v[224:227], v[94:97], v[212:215]\n"
    "v_cvt_pk_f16_f32 v159, v155, v159\n"
    "v_xor_b32_e64 v64, v68, v44\n"
    "v_mfma_f32_16x16x32_f16 v[110:113], v[66:69], v[8:11], v[110:113]\n"
    "v_xor_b32_e64 v63, v67, v44\n"
    "v_cvt_pk_f16_f32 v156, v152, v156\n"
    "v_mfma_f32_16x16x32_f16 v[114:117], v[66:69], v[46:49], v[114:117]\n"
    "v_pk_mul_f16 v153, v157, v35 op_sel:[0,0] op_sel_hi:[1,0]\n"
    "v_pk_mul_f16 v154, v158, v36 op_sel:[0,0] op_sel_hi:[1,0]\n"
    "v_mfma_f32_16x16x32_f16 v[122:125], v[62:65], v[46:49], v[122:125]\n"
    "v_pk_mul_f16 v155, v159, v37 op_sel:[0,0] op_sel_hi:[1,0]\n"
    "v_pk_mul_f16 v152, v156, v34 op_sel:[0,0] op_sel_hi:[1,0]\n"
    "v_mfma_f32_16x16x32_f16 v[118:121], v[62:65], v[8:11], v[118:121]\n"
    "v_pk_fma_f16 v158, v158, v36, v154 op_sel:[1,1,0] op_sel_hi:[0,1,1] neg_lo:[0,1,0]\n"
    "v_pk_fma_f16 v157, v157, v35, v153 op_sel:[1,1,0] op_sel_hi:[0,1,1] neg_lo:[0,1,0]\n"
    "v_pk_fma_f16 v156, v156, v34, v152 op_sel:[1,1,0] op_sel_hi:[0,1,1] neg_lo:[0,1,0]\n"
    "v_pk_fma_f16 v159, v159, v37, v155 op_sel:[1,1,0] op_sel_hi:[0,1,1] neg_lo:[0,1,0]\n"
    "v_cvt_pk_f16_f32 v200, v196, v200\n"
    "v_cvt_pk_f16_f32 v203, v199, v203\n"
    "v_mfma_f32_16x16x32_f16 v[110:113], v[156:159], v[70:73], v[110:113]\n"
    "buffer_load_dwordx4 v76, s[60:63], s58 offen offset:64 lds\n"
    "v_cvt_pk_f16_f32 v202, v198, v202\n"
    "v_mfma_f32_16x16x32_f16 v[114:117], v[156:159], v[106:109], v[114:117]\n"
    "s_add_u32 s58, s71, 0\n"
    "v_xor_b32 v153, v157, v44\n"
    "v_xor_b32 v152, v156, v44\n"
    "v_xor_b32 v154, v158, v44\n"
    "v_pk_mul_f16 v196, v200, v38 op_sel:[0,0] op_sel_hi:[1,0]\n"
    "v_cvt_pk_f16_f32 v201, v197, v201\n"
    "v_xor_b32_e64 v155, v159, v44\n"
    "v_pk_mul_f16 v199, v203, v41 op_sel:[0,0] op_sel_hi:[1,0]\n"
    "v_pk_mul_f16 v198, v202, v40 op_sel:[0,0] op_sel_hi:[1,0]\n"
    "v_mfma_f32_16x16x32_f16 v[122:125], v[152:155], v[106:109], v[122:125]\n"
    "s_add_u32 m0, s37, 0x8000\n"
    "v_pk_mul_f16 v197, v201, v39 op_sel:[0,0] op_sel_hi:[1,0]\n"
    "v_pk_fma_f16 v200, v200, v38, v196 op_sel:[1,1,0] op_sel_hi:[0,1,1] neg_lo:[0,1,0]\n"
    "v_mfma_f32_16x16x32_f16 v[118:121], v[152:155], v[70:73], v[118:121]\n"
    "v_pk_fma_f16 v201, v201, v39, v197 op_sel:[1,1,0] op_sel_hi:[0,1,1] neg_lo:[0,1,0]\n"
    "v_pk_fma_f16 v203, v203, v41, v199 op_sel:[1,1,0] op_sel_hi:[0,1,1] neg_lo:[0,1,0]\n"
    "v_pk_fma_f16 v202, v202, v40, v198 op_sel:[1,1,0] op_sel_hi:[0,1,1] neg_lo:[0,1,0]\n"
    "v_cvt_pk_f16_f32 v218, v214, v218\n"
    "v_cvt_pk_f16_f32 v219, v215, v219\n"
    "v_mfma_f32_16x16x32_f16 v[110:113], v[200:203], v[126:129], v[110:113]\n"
    "v_xor_b32_e64 v197, v201, v44\n"
    "v_cvt_pk_f16_f32 v216, v212, v216\n"
    "v_mfma_f32_16x16x32_f16 v[114:117], v[200:203], v[130:133], v[114:117]\n"
    "v_xor_b32 v196, v200, v44\n"
    "v_xor_b32 v198, v202, v44\n"
    "v_xor_b32_e64 v199, v203, v44\n"
    "v_cvt_pk_f16_f32 v217, v213, v217\n"
    "v_pk_mul_f16 v212, v216, v50 op_sel:[0,0] op_sel_hi:[1,0]\n"
    "v_mfma_f32_16x16x32_f16 v[118:121], v[196:199], v[126:129], v[118:121]\n"
    "v_pk_mul_f16 v214, v218, v52 op_sel:[0,0] op_sel_hi:[1,0]\n"
    "v_pk_mul_f16 v215, v219, v53 op_sel:[0,0] op_sel_hi:[1,0]\n"
    "v_mfma_f32_16x16x32_f16 v[122:125], v[196:199], v[130:133], v[122:125]\n"
    "buffer_load_dwordx4 v76, s[60:63], s58 offen lds\n"
    "v_pk_mul_f16 v213, v217, v51 op_sel:[0,0] op_sel_hi:[1,0]\n"
    "v_pk_fma_f16 v218, v218, v52, v214 op_sel:[1,1,0] op_sel_hi:[0,1,1] neg_lo:[0,1,0]\n"
    "v_pk_fma_f16 v219, v219, v53, v215 op_sel:[1,1,0] op_sel_hi:[0,1,1] neg_lo:[0,1,0]\n"
    "v_pk_fma_f16 v216, v216, v50, v212 op_sel:[1,1,0] op_sel_hi:[0,1,1] neg_lo:[0,1,0]\n"
    "v_pk_fma_f16 v217, v217, v51, v213 op_sel:[1,1,0] op_sel_hi:[0,1,1] neg_lo:[0,1,0]\n"
    "v_xor_b32 v214, v218, v44\n"
    "v_xor_b32 v215, v219, v44\n"
    "v_mfma_f32_16x16x32_f16 v[110:113], v[216:219], v[134:137], v[110:113]\n"
    "s_add_u32 m0, s37, 0xffc0\n"
    "v_xor_b32_e64 v213, v217, v44\n"
    "v_xor_b32 v212, v216, v44\n"
    "v_mfma_f32_16x16x32_f16 v[114:117], v[216:219], v[144:147], v[114:117]\n"
    "buffer_load_dwordx4 v76, s[60:63], s58 offen offset:64 lds\n"
    "s_add_u32 s58, s71, 0x15800\n"
    "v_mfma_f32_16x16x32_f16 v[118:121], v[212:215], v[134:137], v[118:121]\n"
    "s_add_u32 m0, s37, 0x9000\n"
    "v_cvt_pk_bf16_f32 v110, v110, v111\n"
    "v_cvt_pk_bf16_f32 v111, v112, v113\n"
    "v_mfma_f32_16x16x32_f16 v[122:125], v[212:215], v[144:147], v[122:125]\n"
    "s_nop 0\n"
    "v_cvt_pk_bf16_f32 v114, v114, v115\n"
    "v_cvt_pk_bf16_f32 v115, v116, v117\n"
    "v_dot2c_f32_bf16 v184, s42, v110\n"
    "v_cvt_pk_bf16_f32 v118, v118, v119\n"
    "v_dot2c_f32_bf16 v188, s42, v118\n"
    "v_cvt_pk_bf16_f32 v119, v120, v121\n"
    "v_dot2c_f32_bf16 v189, v118, v118\n"
    "v_cvt_pk_bf16_f32 v122, v122, v123\n"
    "v_dot2c_f32_bf16 v190, s42, v122\n"
    "v_dot2c_f32_bf16 v191, v122, v122\n"
    "v_cvt_pk_bf16_f32 v123, v124, v125\n"
    "v_dot2c_f32_bf16 v187, v114, v114\n"
    "v_dot2c_f32_bf16 v186, s42, v114\n"
    "v_dot2c_f32_bf16 v190, s42, v123\n"
    "v_dot2c_f32_bf16 v184, s42, v111\n"
    "v_dot2c_f32_bf16 v191, v123, v123\n"
    "v_dot2c_f32_bf16 v188, s42, v119\n"
    "v_dot2c_f32_bf16 v185, v110, v110\n"
    "v_dot2c_f32_bf16 v189, v119, v119\n"
    "v_dot2c_f32_bf16 v186, s42, v115\n"
    "v_dot2c_f32_bf16 v185, v111, v111\n"
    "v_dot2c_f32_bf16 v187, v115, v115\n"
    "v_add_f32_dpp v188, v188, v188 quad_perm:[1,0,3,2] row_mask:0xf bank_mask:0xf bound_ctrl:1\n"
    "v_add_f32_dpp v190, v190, v190 quad_perm:[1,0,3,2] row_mask:0xf bank_mask:0xf bound_ctrl:1\n"
    "v_add_f32_dpp v191, v191, v191 quad_perm:[1,0,3,2] row_mask:0xf bank_mask:0xf bound_ctrl:1\n"
    "v_add_f32_dpp v184, v184, v184 quad_perm:[1,0,3,2] row_mask:0xf bank_mask:0xf bound_ctrl:1\n"
    "v_add_f32_dpp v185, v185, v185 quad_perm:[1,0,3,2] row_mask:0xf bank_mask:0xf bound_ctrl:1\n"
    "v_add_f32_dpp v186, v186, v186 quad_perm:[1,0,3,2] row_mask:0xf bank_mask:0xf bound_ctrl:1\n"
    "v_add_f32_dpp v189, v189, v189 quad_perm:[1,0,3,2] row_mask:0xf bank_mask:0xf bound_ctrl:1\n"
    "v_add_f32_dpp v187, v187, v187 quad_perm:[1,0,3,2] row_mask:0xf bank_mask:0xf bound_ctrl:1\n"
    "v_add_f32_dpp v188, v188, v188 quad_perm:[2,3,0,1] row_mask:0xf bank_mask:0xf bound_ctrl:1\n"
    "v_add_f32_dpp v190, v190, v190 quad_perm:[2,3,0,1] row_mask:0xf bank_mask:0xf bound_ctrl:1\n"
    "v_add_f32_dpp v191, v191, v191 quad_perm:[2,3,0,1] row_mask:0xf bank_mask:0xf bound_ctrl:1\n"
    "v_add_f32_dpp v186, v186, v186 quad_perm:[2,3,0,1] row_mask:0xf bank_mask:0xf bound_ctrl:1\n"
    "v_add_f32_dpp v184, v184, v184 quad_perm:[2,3,0,1] row_mask:0xf bank_mask:0xf bound_ctrl:1\n"
    "v_add_f32_dpp v187, v187, v187 quad_perm:[2,3,0,1] row_mask:0xf bank_mask:0xf bound_ctrl:1\n"
    "v_add_f32_dpp v189, v189, v189 quad_perm:[2,3,0,1] row_mask:0xf bank_mask:0xf bound_ctrl:1\n"
    "v_add_f32_dpp v185, v185, v185 quad_perm:[2,3,0,1] row_mask:0xf bank_mask:0xf bound_ctrl:1\n"
    "v_add_f32_dpp v184, v184, v184 row_half_mirror row_mask:0xf bank_mask:0xf bound_ctrl:1\n"
    "v_add_f32_dpp v190, v190, v190 row_half_mirror row_mask:0xf bank_mask:0xf bound_ctrl:1\n"
    "v_add_f32_dpp v188, v188, v188 row_half_mirror row_mask:0xf bank_mask:0xf bound_ctrl:1\n"
    "v_add_f32_dpp v189, v189, v189 row_half_mirror row_mask:0xf bank_mask:0xf bound_ctrl:1\n"
    "v_add_f32_dpp v186, v186, v186 row_half_mirror row_mask:0xf bank_mask:0xf bound_ctrl:1\n"
    "v_add_f32_dpp v191, v191, v191 row_half_mirror row_mask:0xf bank_mask:0xf bound_ctrl:1\n"
    "v_add_f32_dpp v187, v187, v187 row_half_mirror row_mask:0xf bank_mask:0xf bound_ctrl:1\n"
    "v_add_f32_dpp v185, v185, v185 row_half_mirror row_mask:0xf bank_mask:0xf bound_ctrl:1\n"
    "buffer_load_dwordx4 v76, s[60:63], s58 offen lds\n"
    "s_add_u32 m0, s37, 0x10fc0\n"
    "ds_write_b64 v176, v[118:119]\n"
    "ds_write_b64 v177, v[122:123]\n"
    "ds_write_b64 v174, v[110:111]\n"
    "ds_write_b64 v175, v[114:115]\n"
    "buffer_load_dwordx4 v76, s[60:63], s58 offen offset:64 lds\n"
    "s_mov_b32 exec_lo, 0x1010101\n"
    "s_mov_b32 exec_hi, 0x1010101\n"
    "ds_write_b64 v178, v[184:185] offset:0\n"
    "ds_write_b64 v178, v[186:187] offset:32\n"
    "ds_write_b64 v178, v[188:189] offset:64\n"
    "ds_write_b64 v178, v[190:191] offset:96\n"
    "s_mov_b64 exec, -1\n"
    "s_waitcnt lgkmcnt(0)\n"
    "s_barrier\n"
    "ds_read_b128 v[6:9], v179\n"
    "ds_read_b128 v[54:57], v180\n"
    "s_add_u32 s58, s71, 0x2b000\n"
    "s_add_u32 m0, s37, 0xa000\n"
    "ds_read_b128 v[58:61], v180 offset:4096\n"
    "buffer_load_dwordx4 v76, s[60:63], s58 offen lds\n"
    "s_add_u32 m0, s37, 0x11fc0\n"
    "s_nop 0\n"
    "buffer_load_dwordx4 v76, s[60:63], s58 offen offset:64 lds\n"
    "s_waitcnt lgkmcnt(2)\n"
    "v_add_f32 v6, v6, v8\n"
    "v_add_f32 v7, v7, v9\n"
    "s_nop 0\n"
    "v_add_f32_dpp v6, v6, v6 row_ror:8 row_mask:0xf bank_mask:0xf bound_ctrl:1\n"
    "v_add_f32_dpp v7, v7, v7 row_ror:8 row_mask:0xf bank_mask:0xf bound_ctrl:1\n"
    "v_mov_b32 v2, v6\n"
    "v_mov_b32 v11, v7\n"
    "s_waitcnt lgkmcnt(1)\n"
    "v_permlane16_swap_b32 v6, v2\n"
    "v_permlane16_swap_b32 v7, v11\n"
    "v_add_f32 v6, v6, v2\n"
    "v_add_f32 v7, v7, v11\n"
    "v_mov_b32 v10, v6\n"
    "v_mov_b32 v12, v7\n"
    "v_lshlrev_b32 v42, 16, v54\n"
    "v_permlane32_swap_b32 v6, v10\n"
    "v_permlane32_swap_b32 v7, v12\n"
    "v_add_f32 v6, v6, v10\n"
    "v_add_f32 v7, v7, v12\n"
    "v_mul_f32 v13, 0x3b000000, v6\n"
    "v_mul_f32 v26, 0x3b000000, v7\n"
    "v_fma_f32 v26, -v13, v13, v26\n"
    "v_max_f32_e64 v26, 0, v26\n"
    "v_cvt_pk_bf16_f32 v26, v26, v13\n"
    "v_and_b32 v13, 0xffff0000, v26\n"
    "v_lshlrev_b32_e64 v26, 16, v26\n"
    "v_add_f32 v26, 0x3727c5ac, v26\n"
    "v_cvt_pk_bf16_f32 v26, v26, v26\n"
    "v_lshlrev_b32 v26, 16, v26\n"
    "v_sqrt_f32 v26, v26\n"
    "v_and_b32 v43, 0xffff0000, v54\n"
    "v_cvt_pk_bf16_f32 v26, v26, v26\n"
    "v_lshlrev_b32 v26, 16, v26\n"
    "v_rcp_f32 v26, v26\n"
    "v_lshlrev_b32_e64 v46, 16, v56\n"
    "v_mul_f32 v30, 0x3eeff558, v26\n"
    "v_mul_f32_e64 v32, -v13, v30\n"
    "v_and_b32 v47, 0xffff0000, v56\n"
    "v_pk_fma_f32 v[42:43], v[42:43], v[30:31], v[32:33] op_sel_hi:[1,0,0]\n"
    "v_lshlrev_b32_e64 v44, 16, v55\n"
    "v_and_b32 v45, 0xffff0000, v55\n"
    "v_pk_fma_f32 v[46:47], v[46:47], v[30:31], v[32:33] op_sel_hi:[1,0,0]\n"
    "v_lshlrev_b32_e64 v48, 16, v57\n"
    "v_and_b32 v49, 0xffff0000, v57\n"
    "v_cvt_pk_bf16_f32 v34, v42, v43\n"
    "v_pk_fma_f32 v[44:45], v[44:45], v[30:31], v[32:33] op_sel_hi:[1,0,0]\n"
    "v_cvt_pk_bf16_f32 v36, v46, v47\n"
    "v_pk_fma_f32 v[48:49], v[48:49], v[30:31], v[32:33] op_sel_hi:[1,0,0]\n"
    "v_cvt_pk_bf16_f32 v35, v44, v45\n"
    "v_cvt_pk_bf16_f32 v37, v48, v49\n"
    "v_min_u32_e64 v34, -2, v34\n"
    "v_min_u32 v36, -2, v36\n"
    "s_waitcnt lgkmcnt(0)\n"
    "global_store_dwordx4 v4, v[34:37], s[52:53] offset:-4096 sc1\n"
    "v_lshlrev_b32_e64 v50, 16, v58\n"
    "v_and_b32 v51, 0xffff0000, v58\n"
    "v_lshlrev_b32_e64 v62, 16, v60\n"
    "v_and_b32 v63, 0xffff0000, v60\n"
    "v_pk_fma_f32 v[50:51], v[50:51], v[30:31], v[32:33] op_sel_hi:[1,0,0]\n"
    "v_lshlrev_b32_e64 v52, 16, v59\n"
    "v_and_b32 v53, 0xffff0000, v59\n"
    "v_pk_fma_f32 v[62:63], v[62:63], v[30:31], v[32:33] op_sel_hi:[1,0,0]\n"
    "v_lshlrev_b32_e64 v64, 16, v61\n"
    "v_and_b32 v65, 0xffff0000, v61\n"
    "v_cvt_pk_bf16_f32 v38, v50, v51\n"
    "v_pk_fma_f32 v[52:53], v[52:53], v[30:31], v[32:33] op_sel_hi:[1,0,0]\n"
    "v_cvt_pk_bf16_f32 v40, v62, v63\n"
    "v_pk_fma_f32 v[64:65], v[64:65], v[30:31], v[32:33] op_sel_hi:[1,0,0]\n"
    "v_cvt_pk_bf16_f32 v39, v52, v53\n"
    "v_cvt_pk_bf16_f32 v41, v64, v65\n"
    "v_min_u32_e64 v38, -2, v38\n"
    "v_min_u32 v40, -2, v40\n"
    "global_store_dwordx4 v4, v[38:41], s[52:53] sc1\n"
    "s_nop 0\n"
    "s_or_b32 s28, s2, s27\n"
    "s_cmp_lg_u32 s28, 0\n"
    "s_cbranch_scc1 .L9_%=\n"
    "s_add_u32 s28, s34, 1\n"
    "v_mov_b32 v3, s28\n"
    "s_mov_b64 exec, 1\n"
    "global_store_dword v0, v3, s[18:19] sc1\n"
    "s_mov_b64 exec, -1\n"
    ".L9_%=:\n"
    "s_add_u32 s58, s71, 0x40800\n"
    "s_add_u32 m0, s37, 0xb000\n"
    "s_nop 0\n"
    "buffer_load_dwordx4 v76, s[60:63], s58 offen lds\n"
    "s_add_u32 m0, s37, 0x12fc0\n"
    "s_mul_i32 s28, s27, 0x2200\n"
    "v_mul_u32_u24 v175, 17, v14\n"
    "v_lshrrev_b32 v174, 3, v5\n"
    "v_lshlrev_b32_e64 v245, 1, v16\n"
    "v_add_lshl_u32 v175, v175, v15, 4\n"
    "buffer_load_dwordx4 v76, s[60:63], s58 offen offset:64 lds\n"
    "s_add_u32 s58, s72, 0\n"
    "s_add_u32 m0, s37, 0xc000\n"
    "s_nop 0\n"
    "buffer_load_dwordx4 v76, s[60:63], s58 offen lds\n"
    "s_add_u32 m0, s37, 0x13fc0\n"
    "v_lshl_add_u32 v245, v174, 4, v245\n"
    "s_mul_i32 s30, s75, 0xfffff800\n"
    "buffer_load_dwordx4 v76, s[60:63], s58 offen offset:64 lds\n"
    "s_add_u32 s58, s72, 0x15800\n"
    "s_add_u32 m0, s37, 0xd000\n"
    "s_nop 0\n"
    "buffer_load_dwordx4 v76, s[60:63], s58 offen lds\n"
    "s_add_u32 m0, s37, 0x14fc0\n"
    "v_lshrrev_b32 v173, 2, v5\n"
    "v_and_b32 v172, 0x70, v4\n"
    "v_lshl_add_u32 v168, v173, 6, v170\n"
    "buffer_load_dwordx4 v76, s[60:63], s58 offen offset:64 lds\n"
    "s_add_u32 s58, s72, 0x2b000\n"
    "s_add_u32 m0, s37, 0xe000\n"
    "s_nop 0\n"
    "buffer_load_dwordx4 v76, s[60:63], s58 offen lds\n"
    "s_add_u32 m0, s37, 0x15fc0\n"
    "v_lshl_add_u32 v172, v23, 13, v172\n"
    "v_lshl_add_u32 v168, v25, 5, v168\n"
    "v_lshl_add_u32 v2, v15, 7, v172\n"
    "v_xor_b32 v169, 32, v168\n"
    "buffer_load_dwordx4 v76, s[60:63], s58 offen offset:64 lds\n"
    "s_add_u32 s58, s72, 0x40800\n"
    "s_add_u32 m0, s37, 0xf000\n"
    "s_nop 0\n"
    "buffer_load_dwordx4 v76, s[60:63], s58 offen lds\n"
    "s_add_u32 m0, s37, 0x16fc0\n"
    "s_add_u32 s0, s68, s27\n"
    "s_and_b32 s0, s0, 3\n"
    "s_add_u32 s28, s28, 0x4000\n"
    "s_xor_b32 s29, s27, 1\n"
    "s_mul_i32 s29, s29, 0x2200\n"
    "s_add_u32 s29, s29, 0x5100\n"
    "s_and_b32 s31, s26, 7\n"
    "v_add_u32 v28, s28, v175\n"
    "v_add_u32 v245, s28, v245\n"
    "s_lshl_b32 s31, s31, 7\n"
    "v_add_u32_e64 v244, s29, v175\n"
    "s_lshr_b32 s28, s26, 3\n"
    "s_lshl2_add_u32 s29, s28, s27\n"
    "s_lshl_b32 s74, s29, 11\n"
    "s_add_u32 s74, s74, s31\n"
    "s_add_u32 s4, s16, s74\n"
    "s_addc_u32 s5, s17, 0\n"
    "s_lshl_b32 s74, s74, 3\n"
    "s_add_u32 s74, s74, 0x610000\n"
    "s_add_u32 s52, s18, s74\n"
    "s_addc_u32 s53, s19, 0\n"
    "s_lshl1_add_u32 s28, s28, s79\n"
    "s_lshl_b32 s28, s28, 15\n"
    "s_mul_i32 s29, s75, 0x6000\n"
    "s_add_u32 s29, s29, s28\n"
    "s_add_u32 s14, s18, s29\n"
    "s_addc_u32 s15, s19, 0\n"
    "s_mul_i32 s29, s75, 0xffffe000\n"
    "s_add_u32 s29, s29, s28\n"
    "s_add_u32 s29, s29, 0x4000\n"
    "s_add_u32 s20, s18, s29\n"
    "s_addc_u32 s21, s19, 0\n"
    "s_add_u32 s6, s14, 0x811000\n"
    "s_addc_u32 s7, s15, 0\n"
    "s_add_u32 s8, s20, 0x811000\n"
    "s_addc_u32 s9, s21, 0\n"
    "s_add_u32 s10, s14, 0xa11000\n"
    "s_addc_u32 s11, s15, 0\n"
    "s_add_u32 s12, s20, 0xa11000\n"
    "s_addc_u32 s13, s21, 0\n"
    "s_add_u32 s14, s14, 0xc11000\n"
    "s_addc_u32 s15, s15, 0\n"
    "s_add_u32 s20, s20, 0xc11000\n"
    "s_addc_u32 s21, s21, 0\n"
    "s_add_u32 s22, s54, 0x810000\n"
    "s_addc_u32 s23, s55, 0\n"
    "s_add_u32 s38, s54, 0xa10000\n"
    "s_addc_u32 s39, s55, 0\n"
    "s_add_u32 s40, s54, 0xc10000\n"
    "buffer_load_dwordx4 v76, s[60:63], s58 offen offset:64 lds\n"
    "s_addc_u32 s41, s55, 0\n"
    "s_add_u32 s60, s54, 0x814000\n"
    "s_addc_u32 s61, s55, 0\n"
    "s_add_u32 s62, s54, 0xa14000\n"
    "s_addc_u32 s63, s55, 0\n"
    "s_add_u32 s54, s54, 0xc14000\n"
    "s_addc_u32 s55, s55, 0\n"
    "s_lshl2_add_u32 s29, s24, s27\n"
    "s_lshl_b32 s29, s29, 5\n"
    "s_add_u32 s29, s29, s25\n"
    "s_lshl_b32 s29, s29, 8\n"
    "s_add_u32 s29, s29, 0xe10800\n"
    "s_add_u32 s64, s18, s29\n"
    "s_addc_u32 s65, s19, 0\n"
    "s_lshr_b32 s29, s26, 3\n"
    "s_lshl2_add_u32 s29, s29, s27\n"
    "s_lshl_b32 s29, s29, 12\n"
    "s_add_u32 s29, s29, s30\n"
    "s_add_u32 s29, s29, 0xe10800\n"
    "s_add_u32 s66, s18, s29\n"
    "s_addc_u32 s67, s19, 0\n"
    "s_cmp_lt_u32 s25, 15\n"
    "s_cselect_b32 s34, -1, 0\n"
    "v_and_b32 v27, 0x70, v4\n"
    "s_cmp_lt_u32 s25, 14\n"
    "v_add_u32 v17, 0x4000, v2\n"
    "s_mul_i32 s0, s0, 0x3000\n"
    "s_mul_i32 s1, s27, 0x3000\n"
    "v_add_u32 v22, 0x10000, v168\n"
    "v_add_u32 v171, 0x10000, v169\n"
    "v_add_u32 v74, 0xffff0000, v82\n"
    "v_add_u32 v85, 0x10000, v82\n"
    "v_add_u32 v75, 0xffff0000, v83\n"
    "v_add_u32 v160, 0x10000, v83\n"
    "v_add_u32 v77, 0xffff0000, v86\n"
    "v_add_u32 v161, 0x10000, v86\n"
    "v_add_u32 v84, 0xffff0000, v87\n"
    "v_add_u32 v162, 0x10000, v87\n"
    "v_lshl_add_u32 v26, v163, 7, v172\n"
    "v_lshl_add_u32 v27, v174, 18, v27\n"
    "v_lshlrev_b32_e64 v246, 2, v5\n"
    "s_cselect_b32 s36, -1, 0\n"
    "s_mov_b64 exec, 0\n"
    "s_cmp_eq_u32 s27, 0\n"
    "s_cbranch_scc1 .Lpg0_%=\n"
    "s_cmp_eq_u32 s27, 1\n"
    "s_cbranch_scc1 .Lpg1_%=\n"
    "s_cmp_eq_u32 s27, 2\n"
    "s_cbranch_scc1 .Lpg2_%=\n"
    "s_cmp_eq_u32 s27, 3\n"
    "s_cbranch_scc1 .Lpg3_%=\n"
    ".Lwarm_%=:\n"
    "s_mov_b64 exec, -1\n"
    "s_sleep 1\n"
    "s_mov_b32 s70, 0x409d30fc\n"
    "s_mov_b32 s71, 0x409d30fc\n"
    "buffer_load_dwordx4 v[6:9], v2, s[84:87], s81 offen offset:0 sc1\n"
    "buffer_load_dwordx4 v[100:103], v17, s[84:87], s81 offen offset:0 sc1\n"
    "buffer_load_dwordx4 v[10:13], v2, s[84:87], s81 offen offset:512 sc1\n"
    "buffer_load_dwordx4 v[104:107], v17, s[84:87], s81 offen offset:512 sc1\n"
    "buffer_load_dwordx4 v[18:21], v2, s[84:87], s81 offen offset:1024 sc1\n"
    "buffer_load_dwordx4 v[108:111], v17, s[84:87], s81 offen offset:1024 sc1\n"
    "buffer_load_dwordx4 v[30:33], v2, s[84:87], s81 offen offset:1536 sc1\n"
    "buffer_load_dwordx4 v[112:115], v17, s[84:87], s81 offen offset:1536 sc1\n"
    "buffer_load_dwordx4 v[34:37], v2, s[84:87], s82 offen offset:0 sc1\n"
    "buffer_load_dwordx4 v[116:119], v17, s[84:87], s82 offen offset:0 sc1\n"
    "s_waitcnt vmcnt(10)\n"
    "s_barrier\n"
    "ds_read_b64_tr_b16 a[128:129], v168 offset:0\n"
    "ds_read_b64_tr_b16 a[130:131], v168 offset:1024\n"
    "ds_read_b64_tr_b16 a[132:133], v168 offset:2048\n"
    "ds_read_b64_tr_b16 a[134:135], v168 offset:3072\n"
    "ds_read_b64_tr_b16 a[136:137], v169 offset:0\n"
    "ds_read_b64_tr_b16 a[138:139], v169 offset:1024\n"
    "ds_read_b64_tr_b16 a[140:141], v169 offset:2048\n"
    "ds_read_b64_tr_b16 a[142:143], v169 offset:3072\n"
    "ds_read_b64_tr_b16 a[144:145], v22 offset:32768\n"
    "ds_read_b64_tr_b16 a[146:147], v22 offset:33792\n"
    "ds_read_b64_tr_b16 a[148:149], v22 offset:34816\n"
    "ds_read_b64_tr_b16 a[150:151], v22 offset:35840\n"
    "ds_read_b64_tr_b16 a[152:153], v171 offset:32768\n"
    "ds_read_b64_tr_b16 a[154:155], v171 offset:33792\n"
    "ds_read_b64_tr_b16 a[156:157], v171 offset:34816\n"
    "ds_read_b64_tr_b16 a[158:159], v171 offset:35840\n"
    "s_waitcnt vmcnt(8)\n"
    "v_max3_u32 v3, v6, v8, v100\n"
    "v_max_u32 v3, v3, v102\n"
    "v_cmp_eq_u32_e64 vcc, -1, v3\n"
    "s_cbranch_vccnz .R10_%=\n"
    ".B11_%=:\n"
    "s_waitcnt lgkmcnt(14)\n"
    "v_mfma_f32_16x16x32_bf16 v[172:175], a[128:131], v[6:9], 0\n"
    "ds_read_b64_tr_b16 a[160:161], v168 offset:4096\n"
    "v_mfma_f32_16x16x32_bf16 v[188:191], a[128:131], v[100:103], 0\n"
    "ds_read_b64_tr_b16 a[162:163], v168 offset:5120\n"
    "buffer_load_dwordx4 v[38:41], v2, s[84:87], s82 offen offset:512 sc1\n"
    "s_waitcnt lgkmcnt(14)\n"
    "v_mfma_f32_16x16x32_bf16 v[176:179], a[132:135], v[6:9], 0\n"
    "ds_read_b64_tr_b16 a[164:165], v168 offset:6144\n"
    "v_mfma_f32_16x16x32_bf16 v[192:195], a[132:135], v[100:103], 0\n"
    "ds_read_b64_tr_b16 a[166:167], v168 offset:7168\n"
    "s_waitcnt lgkmcnt(14)\n"
    "v_mfma_f32_16x16x32_bf16 v[180:183], a[136:139], v[6:9], 0\n"
    "ds_read_b64_tr_b16 a[168:169], v169 offset:4096\n"
    "v_mfma_f32_16x16x32_bf16 v[196:199], a[136:139], v[100:103], 0\n"
    "ds_read_b64_tr_b16 a[170:171], v169 offset:5120\n"
    "buffer_load_dwordx4 v[120:123], v17, s[84:87], s82 offen offset:512 sc1\n"
    "s_waitcnt lgkmcnt(14)\n"
    "v_mfma_f32_16x16x32_bf16 v[184:187], a[140:143], v[6:9], 0\n"
    "ds_read_b64_tr_b16 a[172:173], v169 offset:6144\n"
    "v_mfma_f32_16x16x32_bf16 v[200:203], a[140:143], v[100:103], 0\n"
    "s_waitcnt lgkmcnt(7)\n"
    "ds_read_b64_tr_b16 a[174:175], v169 offset:7168\n"
    "v_mfma_f32_16x16x32_bf16 v[204:207], a[144:147], v[6:9], 0\n"
    "ds_read_b64_tr_b16 a[128:129], v168 offset:8192\n"
    "v_mfma_f32_16x16x32_bf16 v[220:223], a[144:147], v[100:103], 0\n"
    "ds_read_b64_tr_b16 a[130:131], v168 offset:9216\n"
    "v_mfma_f32_16x16x32_bf16 v[208:211], a[148:151], v[6:9], 0\n"
    "s_waitcnt vmcnt(8)\n"
    "ds_read_b64_tr_b16 a[132:133], v168 offset:10240\n"
    "buffer_load_dwordx4 v[42:45], v2, s[84:87], s82 offen offset:1024 sc1\n"
    "v_max3_u32 v3, v10, v12, v104\n"
    "v_mfma_f32_16x16x32_bf16 v[224:227], a[148:151], v[100:103], 0\n"
    "ds_read_b64_tr_b16 a[134:135], v168 offset:11264\n"
    "v_max_u32 v3, v3, v106\n"
    "v_mfma_f32_16x16x32_bf16 v[212:215], a[152:155], v[6:9], 0\n"
    "ds_read_b64_tr_b16 a[136:137], v169 offset:8192\n"
    "v_cmp_eq_u32_e64 vcc, -1, v3\n"
    "v_mfma_f32_16x16x32_bf16 v[228:231], a[152:155], v[100:103], 0\n"
    "ds_read_b64_tr_b16 a[138:139], v169 offset:9216\n"
    "v_mfma_f32_16x16x32_bf16 v[216:219], a[156:159], v[6:9], 0\n"
    "s_cbranch_vccnz .R14_%=\n"
    ".B15_%=:\n"
    "ds_read_b64_tr_b16 a[140:141], v169 offset:10240\n"
    "v_mfma_f32_16x16x32_bf16 v[232:235], a[156:159], v[100:103], 0\n"
    "s_waitcnt lgkmcnt(7)\n"
    "ds_read_b64_tr_b16 a[142:143], v169 offset:11264\n"
    "buffer_load_dwordx4 v[124:127], v17, s[84:87], s82 offen offset:1024 sc1\n"
    "v_mfma_f32_16x16x32_bf16 v[172:175], a[160:163], v[10:13], v[172:175]\n"
    "ds_read_b64_tr_b16 a[144:145], v168 offset:12288\n"
    "v_mfma_f32_16x16x32_bf16 v[188:191], a[160:163], v[104:107], v[188:191]\n"
    "ds_read_b64_tr_b16 a[146:147], v168 offset:13312\n"
    "v_mfma_f32_16x16x32_bf16 v[176:179], a[164:167], v[10:13], v[176:179]\n"
    "s_waitcnt vmcnt(8)\n"
    "ds_read_b64_tr_b16 a[148:149], v168 offset:14336\n"
    "v_max3_u32 v3, v18, v20, v108\n"
    "v_mfma_f32_16x16x32_bf16 v[192:195], a[164:167], v[104:107], v[192:195]\n"
    "ds_read_b64_tr_b16 a[150:151], v168 offset:15360\n"
    "v_max_u32 v3, v3, v110\n"
    "v_mfma_f32_16x16x32_bf16 v[180:183], a[168:171], v[10:13], v[180:183]\n"
    "ds_read_b64_tr_b16 a[152:153], v169 offset:12288\n"
    "v_cmp_eq_u32_e64 vcc, -1, v3\n"
    "v_mfma_f32_16x16x32_bf16 v[196:199], a[168:171], v[104:107], v[196:199]\n"
    "ds_read_b64_tr_b16 a[154:155], v169 offset:13312\n"
    "buffer_load_dwordx4 v[46:49], v2, s[84:87], s82 offen offset:1536 sc1\n"
    "v_mfma_f32_16x16x32_bf16 v[184:187], a[172:175], v[10:13], v[184:187]\n"
    "s_cbranch_vccnz .R18_%=\n"
    ".B19_%=:\n"
    "ds_read_b64_tr_b16 a[156:157], v169 offset:14336\n"
    "v_mfma_f32_16x16x32_bf16 v[200:203], a[172:175], v[104:107], v[200:203]\n"
    "s_waitcnt lgkmcnt(7)\n"
    "ds_read_b64_tr_b16 a[158:159], v169 offset:15360\n"
    "buffer_load_dwordx4 v[128:131], v17, s[84:87], s82 offen offset:1536 sc1\n"
    "v_mfma_f32_16x16x32_bf16 v[172:175], a[128:131], v[18:21], v[172:175]\n"
    "ds_read_b64_tr_b16 a[160:161], v168 offset:16384\n"
    "v_mfma_f32_16x16x32_bf16 v[188:191], a[128:131], v[108:111], v[188:191]\n"
    "ds_read_b64_tr_b16 a[162:163], v168 offset:17408\n"
    "v_mfma_f32_16x16x32_bf16 v[176:179], a[132:135], v[18:21], v[176:179]\n"
    "s_waitcnt vmcnt(8)\n"
    "ds_read_b64_tr_b16 a[164:165], v168 offset:18432\n"
    "v_max3_u32 v3, v30, v32, v112\n"
    "v_mfma_f32_16x16x32_bf16 v[192:195], a[132:135], v[108:111], v[192:195]\n"
    "ds_read_b64_tr_b16 a[166:167], v168 offset:19456\n"
    "v_max_u32 v3, v3, v114\n"
    "v_mfma_f32_16x16x32_bf16 v[180:183], a[136:139], v[18:21], v[180:183]\n"
    "ds_read_b64_tr_b16 a[168:169], v169 offset:16384\n"
    "v_cmp_eq_u32_e64 vcc, -1, v3\n"
    "v_mfma_f32_16x16x32_bf16 v[196:199], a[136:139], v[108:111], v[196:199]\n"
    "ds_read_b64_tr_b16 a[170:171], v169 offset:17408\n"
    "buffer_load_dwordx4 v[50:53], v2, s[84:87], s83 offen offset:0 sc1\n"
    "v_mfma_f32_16x16x32_bf16 v[184:187], a[140:143], v[18:21], v[184:187]\n"
    "s_cbranch_vccnz .R22_%=\n"
    ".B23_%=:\n"
    "ds_read_b64_tr_b16 a[172:173], v169 offset:18432\n"
    "v_mfma_f32_16x16x32_bf16 v[200:203], a[140:143], v[108:111], v[200:203]\n"
    "s_waitcnt lgkmcnt(7)\n"
    "ds_read_b64_tr_b16 a[174:175], v169 offset:19456\n"
    "v_mfma_f32_16x16x32_bf16 v[172:175], a[144:147], v[30:33], v[172:175]\n"
    "ds_read_b64_tr_b16 a[128:129], v168 offset:20480\n"
    "buffer_load_dwordx4 v[132:135], v17, s[84:87], s83 offen offset:0 sc1\n"
    "v_mfma_f32_16x16x32_bf16 v[188:191], a[144:147], v[112:115], v[188:191]\n"
    "ds_read_b64_tr_b16 a[130:131], v168 offset:21504\n"
    "v_mfma_f32_16x16x32_bf16 v[176:179], a[148:151], v[30:33], v[176:179]\n"
    "s_waitcnt vmcnt(8)\n"
    "ds_read_b64_tr_b16 a[132:133], v168 offset:22528\n"
    "v_max3_u32 v3, v34, v36, v116\n"
    "v_mfma_f32_16x16x32_bf16 v[192:195], a[148:151], v[112:115], v[192:195]\n"
    "ds_read_b64_tr_b16 a[134:135], v168 offset:23552\n"
    "v_max_u32 v3, v3, v118\n"
    "v_mfma_f32_16x16x32_bf16 v[180:183], a[152:155], v[30:33], v[180:183]\n"
    "ds_read_b64_tr_b16 a[136:137], v169 offset:20480\n"
    "v_cmp_eq_u32_e64 vcc, -1, v3\n"
    "v_mfma_f32_16x16x32_bf16 v[196:199], a[152:155], v[112:115], v[196:199]\n"
    "ds_read_b64_tr_b16 a[138:139], v169 offset:21504\n"
    "global_store_dwordx4 v4, v[54:57], s[90:91] offset:-4096 sc1\n"
    "v_mfma_f32_16x16x32_bf16 v[184:187], a[156:159], v[30:33], v[184:187]\n"
    "s_cbranch_vccnz .R26_%=\n"
    ".B27_%=:\n"
    "ds_read_b64_tr_b16 a[140:141], v169 offset:22528\n"
    "v_mfma_f32_16x16x32_bf16 v[200:203], a[156:159], v[112:115], v[200:203]\n"
    "s_waitcnt lgkmcnt(7)\n"
    "ds_read_b64_tr_b16 a[142:143], v169 offset:23552\n"
    "v_mfma_f32_16x16x32_bf16 v[172:175], a[160:163], v[34:37], v[172:175]\n"
    "ds_read_b64_tr_b16 a[144:145], v22 offset:36864\n"
    "v_mfma_f32_16x16x32_bf16 v[188:191], a[160:163], v[116:119], v[188:191]\n"
    "ds_read_b64_tr_b16 a[146:147], v22 offset:37888\n"
    "global_store_dwordx4 v4, v[58:61], s[90:91] sc1\n"
    "v_mfma_f32_16x16x32_bf16 v[176:179], a[164:167], v[34:37], v[176:179]\n"
    "s_waitcnt vmcnt(8)\n"
    "ds_read_b64_tr_b16 a[148:149], v22 offset:38912\n"
    "v_max3_u32 v3, v38, v40, v120\n"
    "v_mfma_f32_16x16x32_bf16 v[192:195], a[164:167], v[116:119], v[192:195]\n"
    "ds_read_b64_tr_b16 a[150:151], v22 offset:39936\n"
    "v_max_u32 v3, v3, v122\n"
    "v_mfma_f32_16x16x32_bf16 v[180:183], a[168:171], v[34:37], v[180:183]\n"
    "ds_read_b64_tr_b16 a[152:153], v171 offset:36864\n"
    "v_cmp_eq_u32_e64 vcc, -1, v3\n"
    "v_mfma_f32_16x16x32_bf16 v[196:199], a[168:171], v[116:119], v[196:199]\n"
    "ds_read_b64_tr_b16 a[154:155], v171 offset:37888\n"
    "v_mfma_f32_16x16x32_bf16 v[184:187], a[172:175], v[34:37], v[184:187]\n"
    "s_cbranch_vccnz .R30_%=\n"
    ".B31_%=:\n"
    "ds_read_b64_tr_b16 a[156:157], v171 offset:38912\n"
    "v_mfma_f32_16x16x32_bf16 v[200:203], a[172:175], v[116:119], v[200:203]\n"
    "s_waitcnt lgkmcnt(7)\n"
    "ds_read_b64_tr_b16 a[158:159], v171 offset:39936\n"
    "buffer_load_dwordx4 v[62:65], v2, s[84:87], s83 offen offset:512 sc1\n"
    "v_mfma_f32_16x16x32_bf16 v[172:175], a[128:131], v[38:41], v[172:175]\n"
    "ds_read_b64_tr_b16 a[160:161], v168 offset:24576\n"
    "v_mfma_f32_16x16x32_bf16 v[188:191], a[128:131], v[120:123], v[188:191]\n"
    "ds_read_b64_tr_b16 a[162:163], v168 offset:25600\n"
    "v_mfma_f32_16x16x32_bf16 v[176:179], a[132:135], v[38:41], v[176:179]\n"
    "ds_read_b64_tr_b16 a[164:165], v168 offset:26624\n"
    "buffer_load_dwordx4 v[136:139], v17, s[84:87], s83 offen offset:512 sc1\n"
    "v_mfma_f32_16x16x32_bf16 v[192:195], a[132:135], v[120:123], v[192:195]\n"
    "ds_read_b64_tr_b16 a[166:167], v168 offset:27648\n"
    "v_mfma_f32_16x16x32_bf16 v[180:183], a[136:139], v[38:41], v[180:183]\n"
    "ds_read_b64_tr_b16 a[168:169], v169 offset:24576\n"
    "v_mfma_f32_16x16x32_bf16 v[196:199], a[136:139], v[120:123], v[196:199]\n"
    "ds_read_b64_tr_b16 a[170:171], v169 offset:25600\n"
    "v_mfma_f32_16x16x32_bf16 v[184:187], a[140:143], v[38:41], v[184:187]\n"
    "ds_read_b64_tr_b16 a[172:173], v169 offset:26624\n"
    "v_mfma_f32_16x16x32_bf16 v[200:203], a[140:143], v[120:123], v[200:203]\n"
    "s_waitcnt lgkmcnt(7)\n"
    "ds_read_b64_tr_b16 a[174:175], v169 offset:27648\n"
    "buffer_load_dwordx4 v[66:69], v2, s[84:87], s83 offen offset:1024 sc1\n"
    "v_mfma_f32_16x16x32_bf16 v[204:207], a[144:147], v[10:13], v[204:207]\n"
    "ds_read_b64_tr_b16 a[128:129], v22 offset:40960\n"
    "v_mfma_f32_16x16x32_bf16 v[220:223], a[144:147], v[104:107], v[220:223]\n"
    "ds_read_b64_tr_b16 a[130:131], v22 offset:41984\n"
    "v_mfma_f32_16x16x32_bf16 v[208:211], a[148:151], v[10:13], v[208:211]\n"
    "s_waitcnt vmcnt(9)\n"
    "ds_read_b64_tr_b16 a[132:133], v22 offset:43008\n"
    "v_max3_u32 v3, v42, v44, v124\n"
    "v_mfma_f32_16x16x32_bf16 v[224:227], a[148:151], v[104:107], v[224:227]\n"
    "ds_read_b64_tr_b16 a[134:135], v22 offset:44032\n"
    "v_max_u32_e64 v3, v3, v126\n"
    "v_mfma_f32_16x16x32_bf16 v[212:215], a[152:155], v[10:13], v[212:215]\n"
    "ds_read_b64_tr_b16 a[136:137], v171 offset:40960\n"
    "v_cmp_eq_u32_e64 vcc, -1, v3\n"
    "v_mfma_f32_16x16x32_bf16 v[228:231], a[152:155], v[104:107], v[228:231]\n"
    "ds_read_b64_tr_b16 a[138:139], v171 offset:41984\n"
    "buffer_load_dwordx4 v[140:143], v17, s[84:87], s83 offen offset:1024 sc1\n"
    "v_mfma_f32_16x16x32_bf16 v[216:219], a[156:159], v[10:13], v[216:219]\n"
    "s_cbranch_vccnz .R34_%=\n"
    ".B35_%=:\n"
    "ds_read_b64_tr_b16 a[140:141], v171 offset:43008\n"
    "v_mfma_f32_16x16x32_bf16 v[232:235], a[156:159], v[104:107], v[232:235]\n"
    "s_waitcnt lgkmcnt(7)\n"
    "ds_read_b64_tr_b16 a[142:143], v171 offset:44032\n"
    "v_mfma_f32_16x16x32_bf16 v[172:175], a[160:163], v[42:45], v[172:175]\n"
    "ds_read_b64_tr_b16 a[144:145], v168 offset:28672\n"
    "buffer_load_dwordx4 v[70:73], v2, s[84:87], s83 offen offset:1536 sc1\n"
    "v_mfma_f32_16x16x32_bf16 v[188:191], a[160:163], v[124:127], v[188:191]\n"
    "ds_read_b64_tr_b16 a[146:147], v168 offset:29696\n"
    "v_mfma_f32_16x16x32_bf16 v[176:179], a[164:167], v[42:45], v[176:179]\n"
    "ds_read_b64_tr_b16 a[148:149], v168 offset:30720\n"
    "v_mfma_f32_16x16x32_bf16 v[192:195], a[164:167], v[124:127], v[192:195]\n"
    "ds_read_b64_tr_b16 a[150:151], v168 offset:31744\n"
    "v_mfma_f32_16x16x32_bf16 v[180:183], a[168:171], v[42:45], v[180:183]\n"
    "ds_read_b64_tr_b16 a[152:153], v169 offset:28672\n"
    "buffer_load_dwordx4 v[144:147], v17, s[84:87], s83 offen offset:1536 sc1\n"
    "v_mfma_f32_16x16x32_bf16 v[196:199], a[168:171], v[124:127], v[196:199]\n"
    "ds_read_b64_tr_b16 a[154:155], v169 offset:29696\n"
    "v_mfma_f32_16x16x32_bf16 v[184:187], a[172:175], v[42:45], v[184:187]\n"
    "ds_read_b64_tr_b16 a[156:157], v169 offset:30720\n"
    "v_mfma_f32_16x16x32_bf16 v[200:203], a[172:175], v[124:127], v[200:203]\n"
    "s_waitcnt lgkmcnt(7)\n"
    "ds_read_b64_tr_b16 a[158:159], v169 offset:31744\n"
    "v_mfma_f32_16x16x32_bf16 v[204:207], a[128:131], v[18:21], v[204:207]\n"
    "ds_read_b64_tr_b16 a[160:161], v168 offset:32768\n"
    "v_mfma_f32_16x16x32_bf16 v[220:223], a[128:131], v[108:111], v[220:223]\n"
    "ds_read_b64_tr_b16 a[162:163], v168 offset:33792\n"
    "buffer_load_dwordx4 v[78:81], v2, s[84:87], s88 offen offset:0 sc1\n"
    "v_mfma_f32_16x16x32_bf16 v[208:211], a[132:135], v[18:21], v[208:211]\n"
    "s_waitcnt vmcnt(11)\n"
    "ds_read_b64_tr_b16 a[164:165], v168 offset:34816\n"
    "v_max3_u32 v3, v46, v48, v128\n"
    "v_mfma_f32_16x16x32_bf16 v[224:227], a[132:135], v[108:111], v[224:227]\n"
    "ds_read_b64_tr_b16 a[166:167], v168 offset:35840\n"
    "v_max_u32_e64 v3, v3, v130\n"
    "v_mfma_f32_16x16x32_bf16 v[212:215], a[136:139], v[18:21], v[212:215]\n"
    "ds_read_b64_tr_b16 a[168:169], v169 offset:32768\n"
    "v_cmp_eq_u32_e64 vcc, -1, v3\n"
    "v_mfma_f32_16x16x32_bf16 v[228:231], a[136:139], v[108:111], v[228:231]\n"
    "ds_read_b64_tr_b16 a[170:171], v169 offset:33792\n"
    "buffer_load_dwordx4 v[148:151], v17, s[84:87], s88 offen offset:0 sc1\n"
    "v_mfma_f32_16x16x32_bf16 v[216:219], a[140:143], v[18:21], v[216:219]\n"
    "s_cbranch_vccnz .R38_%=\n"
    ".B39_%=:\n"
    "ds_read_b64_tr_b16 a[172:173], v169 offset:34816\n"
    "v_mfma_f32_16x16x32_bf16 v[232:235], a[140:143], v[108:111], v[232:235]\n"
    "s_waitcnt lgkmcnt(7)\n"
    "ds_read_b64_tr_b16 a[174:175], v169 offset:35840\n"
    "v_mfma_f32_16x16x32_bf16 v[172:175], a[144:147], v[46:49], v[172:175]\n"
    "ds_read_b64_tr_b16 a[128:129], v168 offset:36864\n"
    "v_mfma_f32_16x16x32_bf16 v[188:191], a[144:147], v[128:131], v[188:191]\n"
    "ds_read_b64_tr_b16 a[130:131], v168 offset:37888\n"
    "v_mfma_f32_16x16x32_bf16 v[176:179], a[148:151], v[46:49], v[176:179]\n"
    "s_waitcnt vmcnt(10)\n"
    "ds_read_b64_tr_b16 a[132:133], v168 offset:38912\n"
    "buffer_load_dwordx4 v[88:91], v2, s[84:87], s88 offen offset:512 sc1\n"
    "v_max3_u32 v3, v50, v52, v132\n"
    "v_mfma_f32_16x16x32_bf16 v[192:195], a[148:151], v[128:131], v[192:195]\n"
    "ds_read_b64_tr_b16 a[134:135], v168 offset:39936\n"
    "v_max_u32 v3, v3, v134\n"
    "v_mfma_f32_16x16x32_bf16 v[180:183], a[152:155], v[46:49], v[180:183]\n"
    "ds_read_b64_tr_b16 a[136:137], v169 offset:36864\n"
    "v_cmp_eq_u32_e64 vcc, -1, v3\n"
    "v_mfma_f32_16x16x32_bf16 v[196:199], a[152:155], v[128:131], v[196:199]\n"
    "ds_read_b64_tr_b16 a[138:139], v169 offset:37888\n"
    "v_mfma_f32_16x16x32_bf16 v[184:187], a[156:159], v[46:49], v[184:187]\n"
    "s_cbranch_vccnz .R42_%=\n"
    ".B43_%=:\n"
    "ds_read_b64_tr_b16 a[140:141], v169 offset:38912\n"
    "v_mfma_f32_16x16x32_bf16 v[200:203], a[156:159], v[128:131], v[200:203]\n"
    "s_waitcnt lgkmcnt(7)\n"
    "ds_read_b64_tr_b16 a[142:143], v169 offset:39936\n"
    "buffer_load_dwordx4 v[152:155], v17, s[84:87], s88 offen offset:512 sc1\n"
    "v_mfma_f32_16x16x32_bf16 v[172:175], a[160:163], v[50:53], v[172:175]\n"
    "ds_read_b64_tr_b16 a[144:145], v168 offset:40960\n"
    "v_mfma_f32_16x16x32_bf16 v[188:191], a[160:163], v[132:135], v[188:191]\n"
    "ds_read_b64_tr_b16 a[146:147], v168 offset:41984\n"
    "v_mfma_f32_16x16x32_bf16 v[176:179], a[164:167], v[50:53], v[176:179]\n"
    "s_waitcnt vmcnt(8)\n"
    "ds_read_b64_tr_b16 a[148:149], v168 offset:43008\n"
    "v_max3_u32 v3, v62, v64, v136\n"
    "v_mfma_f32_16x16x32_bf16 v[192:195], a[164:167], v[132:135], v[192:195]\n"
    "ds_read_b64_tr_b16 a[150:151], v168 offset:44032\n"
    "v_max_u32 v3, v3, v138\n"
    "v_mfma_f32_16x16x32_bf16 v[180:183], a[168:171], v[50:53], v[180:183]\n"
    "ds_read_b64_tr_b16 a[152:153], v169 offset:40960\n"
    "v_cmp_eq_u32_e64 vcc, -1, v3\n"
    "v_mfma_f32_16x16x32_bf16 v[196:199], a[168:171], v[132:135], v[196:199]\n"
    "ds_read_b64_tr_b16 a[154:155], v169 offset:41984\n"
    "buffer_load_dwordx4 v[92:95], v2, s[84:87], s88 offen offset:1024 sc1\n"
    "v_mfma_f32_16x16x32_bf16 v[184:187], a[172:175], v[50:53], v[184:187]\n"
    "s_cbranch_vccnz .R46_%=\n"
    ".B47_%=:\n"
    "ds_read_b64_tr_b16 a[156:157], v169 offset:43008\n"
    "v_mfma_f32_16x16x32_bf16 v[200:203], a[172:175], v[132:135], v[200:203]\n"
    "s_waitcnt lgkmcnt(7)\n"
    "ds_read_b64_tr_b16 a[158:159], v169 offset:44032\n"
    "buffer_load_dwordx4 v[156:159], v17, s[84:87], s88 offen offset:1024 sc1\n"
    "v_mfma_f32_16x16x32_bf16 v[172:175], a[128:131], v[62:65], v[172:175]\n"
    "ds_read_b64_tr_b16 a[160:161], v168 offset:45056\n"
    "v_mfma_f32_16x16x32_bf16 v[188:191], a[128:131], v[136:139], v[188:191]\n"
    "ds_read_b64_tr_b16 a[162:163], v168 offset:46080\n"
    "v_mfma_f32_16x16x32_bf16 v[176:179], a[132:135], v[62:65], v[176:179]\n"
    "s_waitcnt vmcnt(8)\n"
    "ds_read_b64_tr_b16 a[164:165], v168 offset:47104\n"
    "v_max3_u32 v3, v66, v68, v140\n"
    "v_mfma_f32_16x16x32_bf16 v[192:195], a[132:135], v[136:139], v[192:195]\n"
    "ds_read_b64_tr_b16 a[166:167], v168 offset:48128\n"
    "v_max_u32 v3, v3, v142\n"
    "v_mfma_f32_16x16x32_bf16 v[180:183], a[136:139], v[62:65], v[180:183]\n"
    "ds_read_b64_tr_b16 a[168:169], v169 offset:45056\n"
    "v_cmp_eq_u32_e64 vcc, -1, v3\n"
    "v_mfma_f32_16x16x32_bf16 v[196:199], a[136:139], v[136:139], v[196:199]\n"
    "ds_read_b64_tr_b16 a[170:171], v169 offset:46080\n"
    "buffer_load_dwordx4 v[96:99], v2, s[84:87], s88 offen offset:1536 sc1\n"
    "v_mfma_f32_16x16x32_bf16 v[184:187], a[140:143], v[62:65], v[184:187]\n"
    "s_cbranch_vccnz .R50_%=\n"
    ".B51_%=:\n"
    "ds_read_b64_tr_b16 a[172:173], v169 offset:47104\n"
    "v_mfma_f32_16x16x32_bf16 v[200:203], a[140:143], v[136:139], v[200:203]\n"
    "s_waitcnt lgkmcnt(7)\n"
    "ds_read_b64_tr_b16 a[174:175], v169 offset:48128\n"
    "v_mfma_f32_16x16x32_bf16 v[172:175], a[144:147], v[66:69], v[172:175]\n"
    "ds_read_b64_tr_b16 a[128:129], v168 offset:49152\n"
    "buffer_load_dwordx4 v[164:167], v17, s[84:87], s88 offen offset:1536 sc1\n"
    "v_mfma_f32_16x16x32_bf16 v[188:191], a[144:147], v[140:143], v[188:191]\n"
    "ds_read_b64_tr_b16 a[130:131], v168 offset:50176\n"
    "v_mfma_f32_16x16x32_bf16 v[176:179], a[148:151], v[66:69], v[176:179]\n"
    "s_waitcnt vmcnt(8)\n"
    "ds_read_b64_tr_b16 a[132:133], v168 offset:51200\n"
    "v_max3_u32 v3, v70, v72, v144\n"
    "v_mfma_f32_16x16x32_bf16 v[192:195], a[148:151], v[140:143], v[192:195]\n"
    "s_xor_b32 s43, s59, 0\n"
    "ds_read_b64_tr_b16 a[134:135], v168 offset:52224\n"
    "v_max_u32_e64 v3, v3, v146\n"
    "v_mfma_f32_16x16x32_bf16 v[180:183], a[152:155], v[66:69], v[180:183]\n"
    "s_add_u32 m0, s37, 0x20000\n"
    "ds_read_b64_tr_b16 a[136:137], v169 offset:49152\n"
    "v_cmp_eq_u32_e64 vcc, -1, v3\n"
    "v_mfma_f32_16x16x32_bf16 v[196:199], a[152:155], v[140:143], v[196:199]\n"
    "ds_read_b64_tr_b16 a[138:139], v169 offset:50176\n"
    "buffer_load_dwordx4 v29, s[44:47], s43 offen lds\n"
    "v_mfma_f32_16x16x32_bf16 v[184:187], a[156:159], v[66:69], v[184:187]\n"
    "s_cbranch_vccnz .R54_%=\n"
    ".B55_%=:\n"
    "ds_read_b64_tr_b16 a[140:141], v169 offset:51200\n"
    "v_mfma_f32_16x16x32_bf16 v[200:203], a[156:159], v[140:143], v[200:203]\n"
    "s_waitcnt lgkmcnt(7)\n"
    "ds_read_b64_tr_b16 a[142:143], v169 offset:52224\n"
    "v_mfma_f32_16x16x32_bf16 v[172:175], a[160:163], v[70:73], v[172:175]\n"
    "s_xor_b32 s43, s59, 0x80000\n"
    "ds_read_b64_tr_b16 a[144:145], v168 offset:53248\n"
    "v_mfma_f32_16x16x32_bf16 v[188:191], a[160:163], v[144:147], v[188:191]\n"
    "s_add_u32 m0, s37, 0x21000\n"
    "ds_read_b64_tr_b16 a[146:147], v168 offset:54272\n"
    "buffer_load_dwordx4 v29, s[44:47], s43 offen lds\n"
    "v_mfma_f32_16x16x32_bf16 v[176:179], a[164:167], v[70:73], v[176:179]\n"
    "s_waitcnt vmcnt(8)\n"
    "ds_read_b64_tr_b16 a[148:149], v168 offset:55296\n"
    "v_max3_u32 v3, v78, v80, v148\n"
    "v_mfma_f32_16x16x32_bf16 v[192:195], a[164:167], v[144:147], v[192:195]\n"
    "ds_read_b64_tr_b16 a[150:151], v168 offset:56320\n"
    "v_max_u32 v3, v3, v150\n"
    "v_mfma_f32_16x16x32_bf16 v[180:183], a[168:171], v[70:73], v[180:183]\n"
    "s_xor_b32 s43, s59, 0x10000\n"
    "ds_read_b64_tr_b16 a[152:153], v169 offset:53248\n"
    "v_cmp_eq_u32_e64 vcc, -1, v3\n"
    "v_mfma_f32_16x16x32_bf16 v[196:199], a[168:171], v[144:147], v[196:199]\n"
    "s_add_u32 m0, s37, 0x22000\n"
    "ds_read_b64_tr_b16 a[154:155], v169 offset:54272\n"
    "buffer_load_dwordx4 v29, s[44:47], s43 offen lds\n"
    "v_mfma_f32_16x16x32_bf16 v[184:187], a[172:175], v[70:73], v[184:187]\n"
    "s_cbranch_vccnz .R58_%=\n"
    ".B59_%=:\n"
    "ds_read_b64_tr_b16 a[156:157], v169 offset:55296\n"
    "v_mfma_f32_16x16x32_bf16 v[200:203], a[172:175], v[144:147], v[200:203]\n"
    "s_waitcnt lgkmcnt(7)\n"
    "ds_read_b64_tr_b16 a[158:159], v169 offset:56320\n"
    "v_mfma_f32_16x16x32_bf16 v[172:175], a[128:131], v[78:81], v[172:175]\n"
    "s_xor_b32 s43, s59, 0x90000\n"
    "ds_read_b64_tr_b16 a[160:161], v168 offset:57344\n"
    "v_mfma_f32_16x16x32_bf16 v[188:191], a[128:131], v[148:151], v[188:191]\n"
    "s_add_u32 m0, s37, 0x23000\n"
    "ds_read_b64_tr_b16 a[162:163], v168 offset:58368\n"
    "buffer_load_dwordx4 v29, s[44:47], s43 offen lds\n"
    "v_mfma_f32_16x16x32_bf16 v[176:179], a[132:135], v[78:81], v[176:179]\n"
    "s_waitcnt vmcnt(8)\n"
    "ds_read_b64_tr_b16 a[164:165], v168 offset:59392\n"
    "v_max3_u32 v3, v88, v90, v152\n"
    "v_mfma_f32_16x16x32_bf16 v[192:195], a[132:135], v[148:151], v[192:195]\n"
    "ds_read_b64_tr_b16 a[166:167], v168 offset:60416\n"
    "v_max_u32 v3, v3, v154\n"
    "v_mfma_f32_16x16x32_bf16 v[180:183], a[136:139], v[78:81], v[180:183]\n"
    "ds_read_b64_tr_b16 a[168:169], v169 offset:57344\n"
    "v_cmp_eq_u32_e64 vcc, -1, v3\n"
    "v_mfma_f32_16x16x32_bf16 v[196:199], a[136:139], v[148:151], v[196:199]\n"
    "s_xor_b32 s43, s59, 0x20000\n"
    "ds_read_b64_tr_b16 a[170:171], v169 offset:58368\n"
    "v_mfma_f32_16x16x32_bf16 v[184:187], a[140:143], v[78:81], v[184:187]\n"
    "s_cbranch_vccnz .R62_%=\n"
    ".B63_%=:\n"
    "ds_read_b64_tr_b16 a[172:173], v169 offset:59392\n"
    "v_mfma_f32_16x16x32_bf16 v[200:203], a[140:143], v[148:151], v[200:203]\n"
    "s_waitcnt lgkmcnt(7)\n"
    "ds_read_b64_tr_b16 a[174:175], v169 offset:60416\n"
    "v_mfma_f32_16x16x32_bf16 v[172:175], a[144:147], v[88:91], v[172:175]\n"
    "s_add_u32 m0, s37, 0x24000\n"
    "ds_read_b64_tr_b16 a[128:129], v168 offset:61440\n"
    "buffer_load_dwordx4 v29, s[44:47], s43 offen lds\n"
    "v_mfma_f32_16x16x32_bf16 v[188:191], a[144:147], v[152:155], v[188:191]\n"
    "s_xor_b32 s43, s59, 0xa0000\n"
    "ds_read_b64_tr_b16 a[130:131], v168 offset:62464\n"
    "v_mfma_f32_16x16x32_bf16 v[176:179], a[148:151], v[88:91], v[176:179]\n"
    "s_waitcnt vmcnt(7)\n"
    "ds_read_b64_tr_b16 a[132:133], v168 offset:63488\n"
    "v_max3_u32 v3, v92, v94, v156\n"
    "v_mfma_f32_16x16x32_bf16 v[192:195], a[148:151], v[152:155], v[192:195]\n"
    "s_add_u32 m0, s37, 0x25000\n"
    "ds_read_b64_tr_b16 a[134:135], v168 offset:64512\n"
    "v_max_u32 v3, v3, v158\n"
    "v_mfma_f32_16x16x32_bf16 v[180:183], a[152:155], v[88:91], v[180:183]\n"
    "ds_read_b64_tr_b16 a[136:137], v169 offset:61440\n"
    "v_cmp_eq_u32_e64 vcc, -1, v3\n"
    "v_mfma_f32_16x16x32_bf16 v[196:199], a[152:155], v[152:155], v[196:199]\n"
    "ds_read_b64_tr_b16 a[138:139], v169 offset:62464\n"
    "buffer_load_dwordx4 v29, s[44:47], s43 offen lds\n"
    "v_mfma_f32_16x16x32_bf16 v[184:187], a[156:159], v[88:91], v[184:187]\n"
    "s_cbranch_vccnz .R66_%=\n"
    ".B67_%=:\n"
    "ds_read_b64_tr_b16 a[140:141], v169 offset:63488\n"
    "v_mfma_f32_16x16x32_bf16 v[200:203], a[156:159], v[152:155], v[200:203]\n"
    "s_waitcnt lgkmcnt(7)\n"
    "ds_read_b64_tr_b16 a[142:143], v169 offset:64512\n"
    "v_mfma_f32_16x16x32_bf16 v[172:175], a[160:163], v[92:95], v[172:175]\n"
    "s_xor_b32 s43, s59, 0x30000\n"
    ".Lpg0_%=:\n"
    "s_cbranch_execz .Lpg4_%=\n"
    "ds_read_b64_tr_b16 a[144:145], v22 offset:45056\n"
    "v_mfma_f32_16x16x32_bf16 v[188:191], a[160:163], v[156:159], v[188:191]\n"
    "s_add_u32 m0, s37, 0x26000\n"
    "ds_read_b64_tr_b16 a[146:147], v22 offset:46080\n"
    "buffer_load_dwordx4 v29, s[44:47], s43 offen lds\n"
    "v_mfma_f32_16x16x32_bf16 v[176:179], a[164:167], v[92:95], v[176:179]\n"
    "s_waitcnt vmcnt(7)\n"
    "ds_read_b64_tr_b16 a[148:149], v22 offset:47104\n"
    "v_max3_u32 v3, v96, v98, v164\n"
    "v_mfma_f32_16x16x32_bf16 v[192:195], a[164:167], v[156:159], v[192:195]\n"
    "s_xor_b32 s43, s59, 0xb0000\n"
    "ds_read_b64_tr_b16 a[150:151], v22 offset:48128\n"
    "v_max_u32_e64 v3, v3, v166\n"
    "v_mfma_f32_16x16x32_bf16 v[180:183], a[168:171], v[92:95], v[180:183]\n"
    "s_add_u32 m0, s37, 0x27000\n"
    "ds_read_b64_tr_b16 a[152:153], v171 offset:45056\n"
    "v_cmp_eq_u32_e64 vcc, -1, v3\n"
    "v_mfma_f32_16x16x32_bf16 v[196:199], a[168:171], v[156:159], v[196:199]\n"
    "ds_read_b64_tr_b16 a[154:155], v171 offset:46080\n"
    "buffer_load_dwordx4 v29, s[44:47], s43 offen lds\n"
    "v_mfma_f32_16x16x32_bf16 v[184:187], a[172:175], v[92:95], v[184:187]\n"
    "s_cbranch_vccnz .R70_%=\n"
    ".B71_%=:\n"
    "ds_read_b64_tr_b16 a[156:157], v171 offset:47104\n"
    "v_mfma_f32_16x16x32_bf16 v[200:203], a[172:175], v[156:159], v[200:203]\n"
    "s_waitcnt lgkmcnt(7)\n"
    "ds_read_b64_tr_b16 a[158:159], v171 offset:48128\n"
    "v_mfma_f32_16x16x32_bf16 v[172:175], a[128:131], v[96:99], v[172:175]\n"
    "ds_read_b64_tr_b16 a[160:161], v22 offset:49152\n"
    "v_mfma_f32_16x16x32_bf16 v[188:191], a[128:131], v[164:167], v[188:191]\n"
    "s_barrier\n"
    "ds_read_b64_tr_b16 a[162:163], v22 offset:50176\n"
    "v_mfma_f32_16x16x32_bf16 v[176:179], a[132:135], v[96:99], v[176:179]\n"
    "s_add_u32 s43, s0, 0\n"
    "ds_read_b64_tr_b16 a[164:165], v22 offset:51200\n"
    "v_mfma_f32_16x16x32_bf16 v[192:195], a[132:135], v[164:167], v[192:195]\n"
    "s_add_u32 m0, s1, 0\n"
    "ds_read_b64_tr_b16 a[166:167], v22 offset:52224\n"
    "buffer_load_dwordx4 v16, s[48:51], s43 offen offset:0 sc1 lds\n"
    "v_mfma_f32_16x16x32_bf16 v[180:183], a[136:139], v[96:99], v[180:183]\n"
    "ds_read_b64_tr_b16 a[168:169], v171 offset:49152\n"
    "v_mfma_f32_16x16x32_bf16 v[196:199], a[136:139], v[164:167], v[196:199]\n"
    "ds_read_b64_tr_b16 a[170:171], v171 offset:50176\n"
    "v_mfma_f32_16x16x32_bf16 v[184:187], a[140:143], v[96:99], v[184:187]\n"
    "ds_read_b64_tr_b16 a[172:173], v171 offset:51200\n"
    "v_mfma_f32_16x16x32_bf16 v[200:203], a[140:143], v[164:167], v[200:203]\n"
    "s_waitcnt lgkmcnt(7)\n"
    "ds_read_b64_tr_b16 a[174:175], v171 offset:52224\n"
    "v_mfma_f32_16x16x32_bf16 v[204:207], a[144:147], v[30:33], v[204:207]\n"
    "ds_read_b64_tr_b16 a[128:129], v22 offset:53248\n"
    "buffer_load_dwordx4 v16, s[48:51], s43 offen offset:1024 sc1 lds\n"
    "v_mfma_f32_16x16x32_bf16 v[220:223], a[144:147], v[112:115], v[220:223]\n"
    "ds_read_b64_tr_b16 a[130:131], v22 offset:54272\n"
    "v_pk_fma_f32 v[248:249], v[172:173], v[172:173], s[70:71]\n"
    "v_pk_mul_f32 v[176:177], v[172:173], v[176:177]\n"
    "v_pk_mul_f32 v[172:173], v[172:173], v[248:249]\n"
    "v_pk_fma_f32 v[250:251], v[174:175], v[174:175], s[70:71]\n"
    "v_pk_mul_f32 v[178:179], v[174:175], v[178:179]\n"
    "v_pk_mul_f32 v[174:175], v[174:175], v[250:251]\n"
    "v_pk_fma_f32 v[248:249], v[188:189], v[188:189], s[70:71]\n"
    "v_pk_mul_f32 v[192:193], v[188:189], v[192:193]\n"
    "v_pk_mul_f32 v[188:189], v[188:189], v[248:249]\n"
    "v_pk_fma_f32 v[250:251], v[190:191], v[190:191], s[70:71]\n"
    "v_pk_mul_f32 v[194:195], v[190:191], v[194:195]\n"
    "v_pk_mul_f32 v[190:191], v[190:191], v[250:251]\n"
    "v_pk_fma_f32 v[248:249], v[180:181], v[180:181], s[70:71]\n"
    "v_pk_mul_f32 v[184:185], v[180:181], v[184:185]\n"
    "v_pk_mul_f32 v[180:181], v[180:181], v[248:249]\n"
    "v_pk_fma_f32 v[250:251], v[182:183], v[182:183], s[70:71]\n"
    "v_pk_mul_f32 v[186:187], v[182:183], v[186:187]\n"
    "v_pk_mul_f32 v[182:183], v[182:183], v[250:251]\n"
    "v_pk_fma_f32 v[248:249], v[196:197], v[196:197], s[70:71]\n"
    "v_pk_mul_f32 v[200:201], v[196:197], v[200:201]\n"
    "v_pk_mul_f32 v[196:197], v[196:197], v[248:249]\n"
    "v_pk_fma_f32 v[250:251], v[198:199], v[198:199], s[70:71]\n"
    "v_pk_mul_f32 v[202:203], v[198:199], v[202:203]\n"
    "v_pk_mul_f32 v[198:199], v[198:199], v[250:251]\n"
    "v_mfma_f32_16x16x32_bf16 v[208:211], a[148:151], v[30:33], v[208:211]\n"
    "ds_read_b64_tr_b16 a[132:133], v22 offset:55296\n"
    "v_exp_f32_e64 v172, -v172\n"
    "v_mfma_f32_16x16x32_bf16 v[224:227], a[148:151], v[112:115], v[224:227]\n"
    "ds_read_b64_tr_b16 a[134:135], v22 offset:56320\n"
    "v_exp_f32_e64 v173, -v173\n"
    "v_mfma_f32_16x16x32_bf16 v[212:215], a[152:155], v[30:33], v[212:215]\n"
    "ds_read_b64_tr_b16 a[136:137], v171 offset:53248\n"
    "buffer_load_dwordx4 v16, s[48:51], s43 offen offset:2048 sc1 lds\n"
    "v_mfma_f32_16x16x32_bf16 v[228:231], a[152:155], v[112:115], v[228:231]\n"
    "ds_read_b64_tr_b16 a[138:139], v171 offset:54272\n"
    "v_fma_f32 v172, v172, s89, s89\n"
    "v_fma_f32 v173, v173, s89, s89\n"
    "v_mfma_f32_16x16x32_bf16 v[216:219], a[156:159], v[30:33], v[216:219]\n"
    "ds_read_b64_tr_b16 a[140:141], v171 offset:55296\n"
    "v_rcp_f32 v172, v172\n"
    "v_mfma_f32_16x16x32_bf16 v[232:235], a[156:159], v[112:115], v[232:235]\n"
    "s_waitcnt lgkmcnt(7)\n"
    "ds_read_b64_tr_b16 a[142:143], v171 offset:56320\n"
    "v_rcp_f32_e64 v173, v173\n"
    "v_mfma_f32_16x16x32_bf16 v[204:207], a[160:163], v[34:37], v[204:207]\n"
    "ds_read_b64_tr_b16 a[144:145], v22 offset:57344\n"
    "buffer_load_dwordx4 v16, s[48:51], s43 offen offset:3072 sc1 lds\n"
    "v_mfma_f32_16x16x32_bf16 v[220:223], a[160:163], v[116:119], v[220:223]\n"
    "ds_read_b64_tr_b16 a[146:147], v22 offset:58368\n"
    "v_mul_f32 v172, v172, v176\n"
    "v_mul_f32 v173, v173, v177\n"
    "v_mfma_f32_16x16x32_bf16 v[208:211], a[164:167], v[34:37], v[208:211]\n"
    "s_add_u32 s43, s0, 0x1000\n"
    "ds_read_b64_tr_b16 a[148:149], v22 offset:59392\n"
    "v_cvt_pk_bf16_f32 v236, v172, v173\n"
    "v_mfma_f32_16x16x32_bf16 v[224:227], a[164:167], v[116:119], v[224:227]\n"
    "s_add_u32 m0, s1, 0x1000\n"
    "ds_read_b64_tr_b16 a[150:151], v22 offset:60416\n"
    "buffer_load_dwordx4 v16, s[48:51], s43 offen offset:0 sc1 lds\n"
    "v_exp_f32_e64 v174, -v174\n"
    "v_mfma_f32_16x16x32_bf16 v[212:215], a[168:171], v[34:37], v[212:215]\n"
    "ds_read_b64_tr_b16 a[152:153], v171 offset:57344\n"
    "v_exp_f32_e64 v175, -v175\n"
    "v_mfma_f32_16x16x32_bf16 v[228:231], a[168:171], v[116:119], v[228:231]\n"
    "ds_read_b64_tr_b16 a[154:155], v171 offset:58368\n"
    "v_fma_f32 v174, v174, s89, s89\n"
    "v_mfma_f32_16x16x32_bf16 v[216:219], a[172:175], v[34:37], v[216:219]\n"
    "ds_read_b64_tr_b16 a[156:157], v171 offset:59392\n"
    "v_fma_f32 v175, v175, s89, s89\n"
    "v_mfma_f32_16x16x32_bf16 v[232:235], a[172:175], v[116:119], v[232:235]\n"
    "s_waitcnt lgkmcnt(7)\n"
    "ds_read_b64_tr_b16 a[158:159], v171 offset:60416\n"
    "v_rcp_f32 v174, v174\n"
    "v_mfma_f32_16x16x32_bf16 v[204:207], a[128:131], v[38:41], v[204:207]\n"
    "ds_read_b64_tr_b16 a[160:161], v22 offset:61440\n"
    "buffer_load_dwordx4 v16, s[48:51], s43 offen offset:1024 sc1 lds\n"
    "v_rcp_f32_e64 v175, v175\n"
    "v_mfma_f32_16x16x32_bf16 v[220:223], a[128:131], v[120:123], v[220:223]\n"
    "ds_read_b64_tr_b16 a[162:163], v22 offset:62464\n"
    "v_mul_f32_e64 v174, v174, v178\n"
    "v_mfma_f32_16x16x32_bf16 v[208:211], a[132:135], v[38:41], v[208:211]\n"
    "ds_read_b64_tr_b16 a[164:165], v22 offset:63488\n"
    "v_mul_f32_e64 v175, v175, v179\n"
    "v_cvt_pk_bf16_f32 v237, v174, v175\n"
    "v_mfma_f32_16x16x32_bf16 v[224:227], a[132:135], v[120:123], v[224:227]\n"
    "ds_read_b64_tr_b16 a[166:167], v22 offset:64512\n"
    "v_exp_f32_e64 v180, -v180\n"
    "v_mfma_f32_16x16x32_bf16 v[212:215], a[136:139], v[38:41], v[212:215]\n"
    "ds_read_b64_tr_b16 a[168:169], v171 offset:61440\n"
    "buffer_load_dwordx4 v16, s[48:51], s43 offen offset:2048 sc1 lds\n"
    "v_mfma_f32_16x16x32_bf16 v[228:231], a[136:139], v[120:123], v[228:231]\n"
    "ds_read_b64_tr_b16 a[170:171], v171 offset:62464\n"
    "v_exp_f32_e64 v181, -v181\n"
    "v_mfma_f32_16x16x32_bf16 v[216:219], a[140:143], v[38:41], v[216:219]\n"
    "ds_read_b64_tr_b16 a[172:173], v171 offset:63488\n"
    "v_fma_f32 v180, v180, s89, s89\n"
    "v_fma_f32 v181, v181, s89, s89\n"
    "v_mfma_f32_16x16x32_bf16 v[232:235], a[140:143], v[120:123], v[232:235]\n"
    "s_waitcnt lgkmcnt(7)\n"
    "ds_read_b64_tr_b16 a[174:175], v171 offset:64512\n"
    "v_rcp_f32 v180, v180\n"
    "v_mfma_f32_16x16x32_bf16 v[204:207], a[144:147], v[42:45], v[204:207]\n"
    "ds_read_b64_tr_b16 a[128:129], v22 offset:0\n"
    "buffer_load_dwordx4 v16, s[48:51], s43 offen offset:3072 sc1 lds\n"
    "v_mfma_f32_16x16x32_bf16 v[220:223], a[144:147], v[124:127], v[220:223]\n"
    "ds_read_b64_tr_b16 a[130:131], v22 offset:1024\n"
    "v_rcp_f32_e64 v181, v181\n"
    "v_mfma_f32_16x16x32_bf16 v[208:211], a[148:151], v[42:45], v[208:211]\n"
    "s_add_u32 s43, s0, 0x2000\n"
    "ds_read_b64_tr_b16 a[132:133], v22 offset:2048\n"
    "v_mul_f32 v180, v180, v184\n"
    "v_mul_f32 v181, v181, v185\n"
    "v_mfma_f32_16x16x32_bf16 v[224:227], a[148:151], v[124:127], v[224:227]\n"
    "s_add_u32 m0, s1, 0x2000\n"
    "ds_read_b64_tr_b16 a[134:135], v22 offset:3072\n"
    "buffer_load_dwordx4 v16, s[48:51], s43 offen offset:0 sc1 lds\n"
    "v_cvt_pk_bf16_f32 v238, v180, v181\n"
    "v_mfma_f32_16x16x32_bf16 v[212:215], a[152:155], v[42:45], v[212:215]\n"
    "ds_read_b64_tr_b16 a[136:137], v171 offset:0\n"
    "v_add3_u32 v5, v236, v238, s92\n"
    "v_mfma_f32_16x16x32_bf16 v[228:231], a[152:155], v[124:127], v[228:231]\n"
    "ds_read_b64_tr_b16 a[138:139], v171 offset:1024\n"
    "v_exp_f32_e64 v182, -v182\n"
    "v_mfma_f32_16x16x32_bf16 v[216:219], a[156:159], v[42:45], v[216:219]\n"
    "ds_read_b64_tr_b16 a[140:141], v171 offset:2048\n"
    "v_exp_f32_e64 v183, -v183\n"
    "v_mfma_f32_16x16x32_bf16 v[232:235], a[156:159], v[124:127], v[232:235]\n"
    "s_waitcnt lgkmcnt(7)\n"
    "ds_read_b64_tr_b16 a[142:143], v171 offset:3072\n"
    "v_fma_f32 v182, v182, s89, s89\n"
    "v_fma_f32 v183, v183, s89, s89\n"
    "v_mfma_f32_16x16x32_bf16 v[204:207], a[160:163], v[46:49], v[204:207]\n"
    "ds_read_b64_tr_b16 a[144:145], v22 offset:4096\n"
    "buffer_load_dwordx4 v16, s[48:51], s43 offen offset:1024 sc1 lds\n"
    "v_mfma_f32_16x16x32_bf16 v[220:223], a[160:163], v[128:131], v[220:223]\n"
    "ds_read_b64_tr_b16 a[146:147], v22 offset:5120\n"
    "v_rcp_f32 v182, v182\n"
    "v_mfma_f32_16x16x32_bf16 v[208:211], a[164:167], v[46:49], v[208:211]\n"
    "ds_read_b64_tr_b16 a[148:149], v22 offset:6144\n"
    "v_rcp_f32_e64 v183, v183\n"
    "v_mfma_f32_16x16x32_bf16 v[224:227], a[164:167], v[128:131], v[224:227]\n"
    "ds_read_b64_tr_b16 a[150:151], v22 offset:7168\n"
    "v_mul_f32_e64 v182, v182, v186\n"
    "v_mul_f32 v183, v183, v187\n"
    "v_mfma_f32_16x16x32_bf16 v[212:215], a[168:171], v[46:49], v[212:215]\n"
    "ds_read_b64_tr_b16 a[152:153], v171 offset:4096\n"
    "buffer_load_dwordx4 v16, s[48:51], s43 offen offset:2048 sc1 lds\n"
    "v_mfma_f32_16x16x32_bf16 v[228:231], a[168:171], v[128:131], v[228:231]\n"
    "ds_read_b64_tr_b16 a[154:155], v171 offset:5120\n"
    "v_cvt_pk_bf16_f32 v239, v182, v183\n"
    "v_mfma_f32_16x16x32_bf16 v[216:219], a[172:175], v[46:49], v[216:219]\n"
    "ds_read_b64_tr_b16 a[156:157], v171 offset:6144\n"
    "global_store_dwordx4 v16, v[236:239], s[22:23] sc1\n"
    "v_mfma_f32_16x16x32_bf16 v[232:235], a[172:175], v[128:131], v[232:235]\n"
    "s_waitcnt lgkmcnt(7)\n"
    "ds_read_b64_tr_b16 a[158:159], v171 offset:7168\n"
    "v_exp_f32_e64 v188, -v188\n"
    "v_mfma_f32_16x16x32_bf16 v[204:207], a[128:131], v[50:53], v[204:207]\n"
    "ds_read_b64_tr_b16 a[160:161], v22 offset:8192\n"
    "buffer_load_dwordx4 v16, s[48:51], s43 offen offset:3072 sc1 lds\n"
    "v_exp_f32_e64 v189, -v189\n"
    "v_mfma_f32_16x16x32_bf16 v[220:223], a[128:131], v[132:135], v[220:223]\n"
    "ds_read_b64_tr_b16 a[162:163], v22 offset:9216\n"
    "v_fma_f32 v188, v188, s89, s89\n"
    "v_mfma_f32_16x16x32_bf16 v[208:211], a[132:135], v[50:53], v[208:211]\n"
    "s_xor_b32 s43, s59, 0x40000\n"
    "ds_read_b64_tr_b16 a[164:165], v22 offset:10240\n"
    "v_fma_f32 v189, v189, s89, s89\n"
    "v_rcp_f32_e64 v188, v188\n"
    "v_mfma_f32_16x16x32_bf16 v[224:227], a[132:135], v[132:135], v[224:227]\n"
    "s_add_u32 m0, s37, 0xc000\n"
    "ds_read_b64_tr_b16 a[166:167], v22 offset:11264\n"
    "buffer_load_dwordx4 v29, s[44:47], s43 offen lds\n"
    "v_mfma_f32_16x16x32_bf16 v[212:215], a[136:139], v[50:53], v[212:215]\n"
    "ds_read_b64_tr_b16 a[168:169], v171 offset:8192\n"
    "v_rcp_f32_e64 v189, v189\n"
    "v_mfma_f32_16x16x32_bf16 v[228:231], a[136:139], v[132:135], v[228:231]\n"
    "ds_read_b64_tr_b16 a[170:171], v171 offset:9216\n"
    "v_mul_f32_e64 v188, v188, v192\n"
    "v_mul_f32 v189, v189, v193\n"
    "v_mfma_f32_16x16x32_bf16 v[216:219], a[140:143], v[50:53], v[216:219]\n"
    "s_xor_b32 s43, s59, 0xc0000\n"
    "ds_read_b64_tr_b16 a[172:173], v171 offset:10240\n"
    "v_cvt_pk_bf16_f32 v240, v188, v189\n"
    "v_mfma_f32_16x16x32_bf16 v[232:235], a[140:143], v[132:135], v[232:235]\n"
    "s_waitcnt lgkmcnt(7)\n"
    "ds_read_b64_tr_b16 a[174:175], v171 offset:11264\n"
    "v_exp_f32_e64 v190, -v190\n"
    "v_mfma_f32_16x16x32_bf16 v[204:207], a[144:147], v[62:65], v[204:207]\n"
    "s_add_u32 m0, s37, 0xd000\n"
    "ds_read_b64_tr_b16 a[128:129], v22 offset:12288\n"
    "buffer_load_dwordx4 v29, s[44:47], s43 offen lds\n"
    "v_exp_f32_e64 v191, -v191\n"
    "v_mfma_f32_16x16x32_bf16 v[220:223], a[144:147], v[136:139], v[220:223]\n"
    "ds_read_b64_tr_b16 a[130:131], v22 offset:13312\n"
    "v_fma_f32 v190, v190, s89, s89\n"
    "v_mfma_f32_16x16x32_bf16 v[208:211], a[148:151], v[62:65], v[208:211]\n"
    "s_xor_b32 s43, s59, 0x50000\n"
    "ds_read_b64_tr_b16 a[132:133], v22 offset:14336\n"
    "v_fma_f32 v191, v191, s89, s89\n"
    "v_rcp_f32_e64 v190, v190\n"
    "v_mfma_f32_16x16x32_bf16 v[224:227], a[148:151], v[136:139], v[224:227]\n"
    "s_add_u32 m0, s37, 0xe000\n"
    "ds_read_b64_tr_b16 a[134:135], v22 offset:15360\n"
    "buffer_load_dwordx4 v29, s[44:47], s43 offen lds\n"
    "v_mfma_f32_16x16x32_bf16 v[212:215], a[152:155], v[62:65], v[212:215]\n"
    "ds_read_b64_tr_b16 a[136:137], v171 offset:12288\n"
    "v_rcp_f32_e64 v191, v191\n"
    "v_mfma_f32_16x16x32_bf16 v[228:231], a[152:155], v[136:139], v[228:231]\n"
    "ds_read_b64_tr_b16 a[138:139], v171 offset:13312\n"
    "v_mul_f32_e64 v190, v190, v194\n"
    "v_mul_f32 v191, v191, v195\n"
    "v_mfma_f32_16x16x32_bf16 v[216:219], a[156:159], v[62:65], v[216:219]\n"
    "s_xor_b32 s43, s59, 0xd0000\n"
    "ds_read_b64_tr_b16 a[140:141], v171 offset:14336\n"
    "v_cvt_pk_bf16_f32 v241, v190, v191\n"
    ".Lpg1_%=:\n"
    "s_cbranch_execz .Lwarm_%=\n"
    "v_mfma_f32_16x16x32_bf16 v[232:235], a[156:159], v[136:139], v[232:235]\n"
    "s_waitcnt lgkmcnt(7)\n"
    "ds_read_b64_tr_b16 a[142:143], v171 offset:15360\n"
    "v_exp_f32_e64 v196, -v196\n"
    "v_mfma_f32_16x16x32_bf16 v[204:207], a[160:163], v[66:69], v[204:207]\n"
    "s_add_u32 m0, s37, 0xf000\n"
    "ds_read_b64_tr_b16 a[144:145], v22 offset:16384\n"
    "buffer_load_dwordx4 v29, s[44:47], s43 offen lds\n"
    "v_exp_f32_e64 v197, -v197\n"
    "v_mfma_f32_16x16x32_bf16 v[220:223], a[160:163], v[140:143], v[220:223]\n"
    "ds_read_b64_tr_b16 a[146:147], v22 offset:17408\n"
    "v_fma_f32 v196, v196, s89, s89\n"
    "v_mfma_f32_16x16x32_bf16 v[208:211], a[164:167], v[66:69], v[208:211]\n"
    "s_xor_b32 s43, s59, 0x60000\n"
    "ds_read_b64_tr_b16 a[148:149], v22 offset:18432\n"
    "v_fma_f32 v197, v197, s89, s89\n"
    "v_rcp_f32 v196, v196\n"
    "v_mfma_f32_16x16x32_bf16 v[224:227], a[164:167], v[140:143], v[224:227]\n"
    "s_add_u32 m0, s37, 0x18000\n"
    "ds_read_b64_tr_b16 a[150:151], v22 offset:19456\n"
    "buffer_load_dwordx4 v29, s[44:47], s43 offen lds\n"
    "v_mfma_f32_16x16x32_bf16 v[212:215], a[168:171], v[66:69], v[212:215]\n"
    "ds_read_b64_tr_b16 a[152:153], v171 offset:16384\n"
    "v_rcp_f32_e64 v197, v197\n"
    "v_mfma_f32_16x16x32_bf16 v[228:231], a[168:171], v[140:143], v[228:231]\n"
    "ds_read_b64_tr_b16 a[154:155], v171 offset:17408\n"
    "v_mul_f32 v196, v196, v200\n"
    "v_mul_f32 v197, v197, v201\n"
    "v_mfma_f32_16x16x32_bf16 v[216:219], a[172:175], v[66:69], v[216:219]\n"
    "s_xor_b32 s43, s59, 0xe0000\n"
    "ds_read_b64_tr_b16 a[156:157], v171 offset:18432\n"
    "v_cvt_pk_bf16_f32 v242, v196, v197\n"
    "v_mfma_f32_16x16x32_bf16 v[232:235], a[172:175], v[140:143], v[232:235]\n"
    "s_waitcnt lgkmcnt(7)\n"
    "ds_read_b64_tr_b16 a[158:159], v171 offset:19456\n"
    "v_add3_u32 v5, v240, v242, v5\n"
    "v_exp_f32_e64 v198, -v198\n"
    "v_mfma_f32_16x16x32_bf16 v[204:207], a[128:131], v[70:73], v[204:207]\n"
    "s_barrier\n"
    "ds_read_b64_tr_b16 a[160:161], v22 offset:20480\n"
    "v_exp_f32_e64 v199, -v199\n"
    "v_mfma_f32_16x16x32_bf16 v[220:223], a[128:131], v[144:147], v[220:223]\n"
    "s_add_u32 m0, s37, 0x19000\n"
    "ds_read_b64_tr_b16 a[162:163], v22 offset:21504\n"
    "buffer_load_dwordx4 v29, s[44:47], s43 offen lds\n"
    "v_mfma_f32_16x16x32_bf16 v[208:211], a[132:135], v[70:73], v[208:211]\n"
    "s_xor_b32 s43, s59, 0x70000\n"
    "ds_read_b64_tr_b16 a[164:165], v22 offset:22528\n"
    "v_fma_f32 v198, v198, s89, s89\n"
    "v_fma_f32 v199, v199, s89, s89\n"
    "v_mfma_f32_16x16x32_bf16 v[224:227], a[132:135], v[144:147], v[224:227]\n"
    "s_add_u32 m0, s37, 0x1a000\n"
    "ds_read_b64_tr_b16 a[166:167], v22 offset:23552\n"
    "buffer_load_dwordx4 v29, s[44:47], s43 offen lds\n"
    "v_mfma_f32_16x16x32_bf16 v[212:215], a[136:139], v[70:73], v[212:215]\n"
    "ds_read_b64_tr_b16 a[168:169], v171 offset:20480\n"
    "v_rcp_f32_e64 v198, v198\n"
    "v_mfma_f32_16x16x32_bf16 v[228:231], a[136:139], v[144:147], v[228:231]\n"
    "ds_read_b64_tr_b16 a[170:171], v171 offset:21504\n"
    "v_rcp_f32_e64 v199, v199\n"
    "v_mfma_f32_16x16x32_bf16 v[216:219], a[140:143], v[70:73], v[216:219]\n"
    "s_xor_b32 s43, s59, 0xf0000\n"
    "ds_read_b64_tr_b16 a[172:173], v171 offset:22528\n"
    "v_mul_f32 v198, v198, v202\n"
    "v_mul_f32 v199, v199, v203\n"
    "v_mfma_f32_16x16x32_bf16 v[232:235], a[140:143], v[144:147], v[232:235]\n"
    "s_waitcnt lgkmcnt(7)\n"
    "ds_read_b64_tr_b16 a[174:175], v171 offset:23552\n"
    "v_cvt_pk_bf16_f32 v243, v198, v199\n"
    "v_mfma_f32_16x16x32_bf16 v[204:207], a[144:147], v[78:81], v[204:207]\n"
    "s_add_u32 m0, s37, 0x1b000\n"
    "ds_read_b64_tr_b16 a[128:129], v22 offset:24576\n"
    "buffer_load_dwordx4 v29, s[44:47], s43 offen lds\n"
    "global_store_dwordx4 v16, v[240:243], s[60:61] sc1\n"
    "v_mfma_f32_16x16x32_bf16 v[220:223], a[144:147], v[148:151], v[220:223]\n"
    "ds_read_b64_tr_b16 a[130:131], v22 offset:25600\n"
    "global_store_dword v246, v5, s[64:65] offset:-2048 sc1\n"
    "v_mfma_f32_16x16x32_bf16 v[208:211], a[148:151], v[78:81], v[208:211]\n"
    "s_xor_b32 s43, s59, 0x8000\n"
    "ds_read_b64_tr_b16 a[132:133], v22 offset:26624\n"
    "v_mfma_f32_16x16x32_bf16 v[224:227], a[148:151], v[148:151], v[224:227]\n"
    "s_add_u32 m0, s37, 0x1c000\n"
    "ds_read_b64_tr_b16 a[134:135], v22 offset:27648\n"
    "buffer_load_dwordx4 v29, s[44:47], s43 offen lds\n"
    "v_mfma_f32_16x16x32_bf16 v[212:215], a[152:155], v[78:81], v[212:215]\n"
    "ds_read_b64_tr_b16 a[136:137], v171 offset:24576\n"
    "v_mfma_f32_16x16x32_bf16 v[228:231], a[152:155], v[148:151], v[228:231]\n"
    "ds_read_b64_tr_b16 a[138:139], v171 offset:25600\n"
    "v_mfma_f32_16x16x32_bf16 v[216:219], a[156:159], v[78:81], v[216:219]\n"
    "s_xor_b32 s43, s59, 0x88000\n"
    "ds_read_b64_tr_b16 a[140:141], v171 offset:26624\n"
    "v_mfma_f32_16x16x32_bf16 v[232:235], a[156:159], v[148:151], v[232:235]\n"
    "s_waitcnt lgkmcnt(7)\n"
    "ds_read_b64_tr_b16 a[142:143], v171 offset:27648\n"
    "v_mfma_f32_16x16x32_bf16 v[204:207], a[160:163], v[88:91], v[204:207]\n"
    "s_add_u32 m0, s37, 0x1d000\n"
    "ds_read_b64_tr_b16 a[144:145], v22 offset:28672\n"
    "buffer_load_dwordx4 v29, s[44:47], s43 offen lds\n"
    "v_mfma_f32_16x16x32_bf16 v[220:223], a[160:163], v[152:155], v[220:223]\n"
    "ds_read_b64_tr_b16 a[146:147], v22 offset:29696\n"
    "v_mfma_f32_16x16x32_bf16 v[208:211], a[164:167], v[88:91], v[208:211]\n"
    "s_xor_b32 s43, s59, 0x18000\n"
    "ds_read_b64_tr_b16 a[148:149], v22 offset:30720\n"
    "v_mfma_f32_16x16x32_bf16 v[224:227], a[164:167], v[152:155], v[224:227]\n"
    "s_add_u32 m0, s37, 0x1e000\n"
    "ds_read_b64_tr_b16 a[150:151], v22 offset:31744\n"
    "buffer_load_dwordx4 v29, s[44:47], s43 offen lds\n"
    "v_mfma_f32_16x16x32_bf16 v[212:215], a[168:171], v[88:91], v[212:215]\n"
    "ds_read_b64_tr_b16 a[152:153], v171 offset:28672\n"
    "v_mfma_f32_16x16x32_bf16 v[228:231], a[168:171], v[152:155], v[228:231]\n"
    "ds_read_b64_tr_b16 a[154:155], v171 offset:29696\n"
    "v_mfma_f32_16x16x32_bf16 v[216:219], a[172:175], v[88:91], v[216:219]\n"
    "s_xor_b32 s43, s59, 0x98000\n"
    "ds_read_b64_tr_b16 a[156:157], v171 offset:30720\n"
    "v_mfma_f32_16x16x32_bf16 v[232:235], a[172:175], v[152:155], v[232:235]\n"
    "s_waitcnt lgkmcnt(7)\n"
    "ds_read_b64_tr_b16 a[158:159], v171 offset:31744\n"
    "v_mfma_f32_16x16x32_bf16 v[204:207], a[128:131], v[92:95], v[204:207]\n"
    "s_add_u32 m0, s37, 0x1f000\n"
    "v_mfma_f32_16x16x32_bf16 v[220:223], a[128:131], v[156:159], v[220:223]\n"
    "buffer_load_dwordx4 v29, s[44:47], s43 offen lds\n"
    "v_mfma_f32_16x16x32_bf16 v[208:211], a[132:135], v[92:95], v[208:211]\n"
    "s_xor_b32 s43, s59, 0x28000\n"
    "v_mfma_f32_16x16x32_bf16 v[224:227], a[132:135], v[156:159], v[224:227]\n"
    "s_add_u32 m0, s37, 0x10000\n"
    "v_mfma_f32_16x16x32_bf16 v[212:215], a[136:139], v[92:95], v[212:215]\n"
    "buffer_load_dwordx4 v29, s[44:47], s43 offen lds\n"
    "v_mfma_f32_16x16x32_bf16 v[228:231], a[136:139], v[156:159], v[228:231]\n"
    "s_waitcnt vmcnt(28)\n"
    "v_mfma_f32_16x16x32_bf16 v[216:219], a[140:143], v[92:95], v[216:219]\n"
    "s_waitcnt vmcnt(15)\n"
    "v_mfma_f32_16x16x32_bf16 v[232:235], a[140:143], v[156:159], v[232:235]\n"
    "s_waitcnt lgkmcnt(0)\n"
    "v_mfma_f32_16x16x32_bf16 v[204:207], a[144:147], v[96:99], v[204:207]\n"
    "s_barrier\n"
    "ds_read_b128 a[160:163], v16 offset:0\n"
    "v_mfma_f32_16x16x32_bf16 v[220:223], a[144:147], v[164:167], v[220:223]\n"
    "s_xor_b32 s43, s59, 0xa8000\n"
    "ds_read_b128 a[164:167], v16 offset:1024\n"
    "v_mfma_f32_16x16x32_bf16 v[208:211], a[148:151], v[96:99], v[208:211]\n"
    "s_add_u32 m0, s37, 0x11000\n"
    "ds_read_b128 a[168:171], v16 offset:2048\n"
    "buffer_load_dwordx4 v29, s[44:47], s43 offen lds\n"
    "v_mfma_f32_16x16x32_bf16 v[224:227], a[148:151], v[164:167], v[224:227]\n"
    "s_xor_b32 s43, s59, 0x38000\n"
    "ds_read_b128 a[128:131], v16 offset:3072\n"
    "v_mfma_f32_16x16x32_bf16 v[212:215], a[152:155], v[96:99], v[212:215]\n"
    "s_add_u32 m0, s37, 0x12000\n"
    "ds_read_b128 a[132:135], v16 offset:4096\n"
    "buffer_load_dwordx4 v29, s[44:47], s43 offen lds\n"
    "v_mfma_f32_16x16x32_bf16 v[228:231], a[152:155], v[164:167], v[228:231]\n"
    "ds_read_b128 a[136:139], v16 offset:5120\n"
    "v_mfma_f32_16x16x32_bf16 v[216:219], a[156:159], v[96:99], v[216:219]\n"
    "s_xor_b32 s43, s59, 0xb8000\n"
    "v_mfma_f32_16x16x32_bf16 v[232:235], a[156:159], v[164:167], v[232:235]\n"
    "s_waitcnt lgkmcnt(3)\n"
    "v_mfma_f32_16x16x32_bf16 v[172:175], a[160:163], v[6:9], 0\n"
    "s_add_u32 m0, s37, 0x13000\n"
    "ds_read_b128 a[144:147], v16 offset:6144\n"
    "buffer_load_dwordx4 v29, s[44:47], s43 offen lds\n"
    "v_mfma_f32_16x16x32_bf16 v[188:191], a[160:163], v[100:103], 0\n"
    "v_pk_fma_f32 v[248:249], v[204:205], v[204:205], s[70:71]\n"
    "v_pk_mul_f32 v[208:209], v[204:205], v[208:209]\n"
    "v_pk_mul_f32 v[204:205], v[204:205], v[248:249]\n"
    "v_pk_fma_f32 v[250:251], v[206:207], v[206:207], s[70:71]\n"
    "v_pk_mul_f32 v[210:211], v[206:207], v[210:211]\n"
    "v_pk_mul_f32 v[206:207], v[206:207], v[250:251]\n"
    "v_pk_fma_f32 v[248:249], v[220:221], v[220:221], s[70:71]\n"
    "v_pk_mul_f32 v[224:225], v[220:221], v[224:225]\n"
    "v_pk_mul_f32 v[220:221], v[220:221], v[248:249]\n"
    "v_pk_fma_f32 v[250:251], v[222:223], v[222:223], s[70:71]\n"
    "v_pk_mul_f32 v[226:227], v[222:223], v[226:227]\n"
    "v_pk_mul_f32 v[222:223], v[222:223], v[250:251]\n"
    "v_pk_fma_f32 v[248:249], v[212:213], v[212:213], s[70:71]\n"
    "v_pk_mul_f32 v[216:217], v[212:213], v[216:217]\n"
    "v_pk_mul_f32 v[212:213], v[212:213], v[248:249]\n"
    "v_pk_fma_f32 v[250:251], v[214:215], v[214:215], s[70:71]\n"
    "v_pk_mul_f32 v[218:219], v[214:215], v[218:219]\n"
    "v_pk_mul_f32 v[214:215], v[214:215], v[250:251]\n"
    "v_pk_fma_f32 v[248:249], v[228:229], v[228:229], s[70:71]\n"
    "v_pk_mul_f32 v[232:233], v[228:229], v[232:233]\n"
    "v_pk_mul_f32 v[228:229], v[228:229], v[248:249]\n"
    "v_pk_fma_f32 v[250:251], v[230:231], v[230:231], s[70:71]\n"
    "v_pk_mul_f32 v[234:235], v[230:231], v[234:235]\n"
    "v_pk_mul_f32 v[230:231], v[230:231], v[250:251]\n"
    "v_mfma_f32_16x16x32_bf16 v[176:179], a[164:167], v[6:9], 0\n"
    "s_xor_b32 s43, s59, 0x48000\n"
    "ds_read_b128 a[148:151], v16 offset:7168\n"
    "v_exp_f32_e64 v204, -v204\n"
    "v_exp_f32_e64 v205, -v205\n"
    "v_fma_f32 v204, v204, s89, s89\n"
    "v_mfma_f32_16x16x32_bf16 v[192:195], a[164:167], v[100:103], 0\n"
    "s_add_u32 m0, s37, 0x14000\n"
    "v_fma_f32 v205, v205, s89, s89\n"
    "v_rcp_f32 v204, v204\n"
    "v_rcp_f32 v205, v205\n"
    "v_mfma_f32_16x16x32_bf16 v[180:183], a[168:171], v[6:9], 0\n"
    "ds_read_b128 a[152:155], v16 offset:8192\n"
    "buffer_load_dwordx4 v29, s[44:47], s43 offen lds\n"
    "v_mul_f32_e64 v204, v204, v208\n"
    "v_mul_f32 v205, v205, v209\n"
    "v_cvt_pk_bf16_f32 v236, v204, v205\n"
    "v_exp_f32_e64 v206, -v206\n"
    "v_mfma_f32_16x16x32_bf16 v[196:199], a[168:171], v[100:103], 0\n"
    "s_waitcnt lgkmcnt(3)\n"
    "v_exp_f32_e64 v207, -v207\n"
    "v_fma_f32 v206, v206, s89, s89\n"
    "v_fma_f32 v207, v207, s89, s89\n"
    "v_mfma_f32_16x16x32_bf16 v[172:175], a[128:131], v[10:13], v[172:175]\n"
    "s_xor_b32 s43, s59, 0xc8000\n"
    "ds_read_b128 a[160:163], v16 offset:9216\n"
    "v_rcp_f32 v206, v206\n"
    "v_rcp_f32 v207, v207\n"
    "v_mul_f32 v206, v206, v210\n"
    "v_mul_f32 v207, v207, v211\n"
    "v_mfma_f32_16x16x32_bf16 v[188:191], a[128:131], v[104:107], v[188:191]\n"
    "s_add_u32 m0, s37, 0x15000\n"
    "v_cvt_pk_bf16_f32 v237, v206, v207\n"
    "v_exp_f32_e64 v212, -v212\n"
    "v_exp_f32_e64 v213, -v213\n"
    "v_mfma_f32_16x16x32_bf16 v[176:179], a[132:135], v[10:13], v[176:179]\n"
    "ds_read_b128 a[164:167], v16 offset:10240\n"
    "buffer_load_dwordx4 v29, s[44:47], s43 offen lds\n"
    "v_fma_f32 v212, v212, s89, s89\n"
    "v_fma_f32 v213, v213, s89, s89\n"
    "v_rcp_f32_e64 v212, v212\n"
    "v_mfma_f32_16x16x32_bf16 v[192:195], a[132:135], v[104:107], v[192:195]\n"
    "v_rcp_f32 v213, v213\n"
    "v_mul_f32 v212, v212, v216\n"
    "v_mul_f32_e64 v213, v213, v217\n"
    "v_cvt_pk_bf16_f32 v238, v212, v213\n"
    "v_add3_u32 v5, v236, v238, s92\n"
    "v_mfma_f32_16x16x32_bf16 v[180:183], a[136:139], v[10:13], v[180:183]\n"
    "s_xor_b32 s43, s59, 0x58000\n"
    "ds_read_b128 a[168:171], v16 offset:11264\n"
    "v_exp_f32_e64 v214, -v214\n"
    "v_exp_f32_e64 v215, -v215\n"
    "v_fma_f32 v214, v214, s89, s89\n"
    "v_mfma_f32_16x16x32_bf16 v[196:199], a[136:139], v[104:107], v[196:199]\n"
    "s_waitcnt lgkmcnt(3)\n"
    "v_fma_f32 v215, v215, s89, s89\n"
    "v_rcp_f32 v214, v214\n"
    "v_rcp_f32_e64 v215, v215\n"
    "v_mfma_f32_16x16x32_bf16 v[172:175], a[144:147], v[18:21], v[172:175]\n"
    "s_add_u32 m0, s37, 0x16000\n"
    "ds_read_b128 a[128:131], v16 offset:12288\n"
    "buffer_load_dwordx4 v29, s[44:47], s43 offen lds\n"
    "v_mul_f32 v214, v214, v218\n"
    "v_mul_f32 v215, v215, v219\n"
    "v_cvt_pk_bf16_f32 v239, v214, v215\n"
    "v_mfma_f32_16x16x32_bf16 v[188:191], a[144:147], v[108:111], v[188:191]\n"
    "global_store_dwordx4 v16, v[236:239], s[38:39] sc1\n"
    "v_exp_f32_e64 v220, -v220\n"
    "v_exp_f32_e64 v221, -v221\n"
    "v_mfma_f32_16x16x32_bf16 v[176:179], a[148:151], v[18:21], v[176:179]\n"
    "s_xor_b32 s43, s59, 0xd8000\n"
    "ds_read_b128 a[132:135], v16 offset:13312\n"
    "v_fma_f32 v220, v220, s89, s89\n"
    "v_fma_f32 v221, v221, s89, s89\n"
    "v_rcp_f32_e64 v220, v220\n"
    "v_mfma_f32_16x16x32_bf16 v[192:195], a[148:151], v[108:111], v[192:195]\n"
    "s_add_u32 m0, s37, 0x17000\n"
    "v_rcp_f32 v221, v221\n"
    "v_mul_f32 v220, v220, v224\n"
    "v_mul_f32_e64 v221, v221, v225\n"
    "v_cvt_pk_bf16_f32 v240, v220, v221\n"
    "v_mfma_f32_16x16x32_bf16 v[180:183], a[152:155], v[18:21], v[180:183]\n"
    "ds_read_b128 a[136:139], v16 offset:14336\n"
    "buffer_load_dwordx4 v29, s[44:47], s43 offen lds\n"
    "v_exp_f32_e64 v222, -v222\n"
    "v_exp_f32_e64 v223, -v223\n"
    ".Lpg2_%=:\n"
    "s_cbranch_execz .Lwarm_%=\n"
    "v_mfma_f32_16x16x32_bf16 v[196:199], a[152:155], v[108:111], v[196:199]\n"
    "s_waitcnt lgkmcnt(3)\n"
    "v_fma_f32 v222, v222, s89, s89\n"
    "v_fma_f32 v223, v223, s89, s89\n"
    "v_rcp_f32 v222, v222\n"
    "v_rcp_f32 v223, v223\n"
    "v_mfma_f32_16x16x32_bf16 v[172:175], a[160:163], v[30:33], v[172:175]\n"
    "ds_read_b128 a[144:147], v16 offset:15360\n"
    "v_mul_f32 v222, v222, v226\n"
    "v_mul_f32 v223, v223, v227\n"
    "v_cvt_pk_bf16_f32 v241, v222, v223\n"
    "v_mfma_f32_16x16x32_bf16 v[188:191], a[160:163], v[112:115], v[188:191]\n"
    "v_exp_f32_e64 v228, -v228\n"
    "v_exp_f32_e64 v229, -v229\n"
    "v_fma_f32 v228, v228, s89, s89\n"
    "v_fma_f32 v229, v229, s89, s89\n"
    "v_mfma_f32_16x16x32_bf16 v[176:179], a[164:167], v[30:33], v[176:179]\n"
    "ds_read_b128 a[148:151], v16 offset:16384\n"
    "v_rcp_f32 v228, v228\n"
    "v_rcp_f32 v229, v229\n"
    "v_mfma_f32_16x16x32_bf16 v[192:195], a[164:167], v[112:115], v[192:195]\n"
    "v_mul_f32 v228, v228, v232\n"
    "v_mul_f32 v229, v229, v233\n"
    "v_cvt_pk_bf16_f32 v242, v228, v229\n"
    "v_add3_u32 v5, v240, v242, v5\n"
    "v_exp_f32_e64 v230, -v230\n"
    "v_mfma_f32_16x16x32_bf16 v[180:183], a[168:171], v[30:33], v[180:183]\n"
    "ds_read_b128 a[152:155], v16 offset:17408\n"
    "v_exp_f32_e64 v231, -v231\n"
    "v_fma_f32 v230, v230, s89, s89\n"
    "v_fma_f32 v231, v231, s89, s89\n"
    "v_mfma_f32_16x16x32_bf16 v[196:199], a[168:171], v[112:115], v[196:199]\n"
    "s_waitcnt lgkmcnt(3)\n"
    "v_rcp_f32 v230, v230\n"
    "v_rcp_f32 v231, v231\n"
    "v_mul_f32 v230, v230, v234\n"
    "v_mfma_f32_16x16x32_bf16 v[172:175], a[128:131], v[34:37], v[172:175]\n"
    "ds_read_b128 a[160:163], v16 offset:18432\n"
    "v_mul_f32_e64 v231, v231, v235\n"
    "v_cvt_pk_bf16_f32 v243, v230, v231\n"
    "v_mfma_f32_16x16x32_bf16 v[188:191], a[128:131], v[116:119], v[188:191]\n"
    "global_store_dwordx4 v16, v[240:243], s[62:63] sc1\n"
    "v_mfma_f32_16x16x32_bf16 v[176:179], a[132:135], v[34:37], v[176:179]\n"
    "ds_read_b128 a[164:167], v16 offset:19456\n"
    "global_store_dword v246, v5, s[64:65] offset:2048 sc1\n"
    "v_mfma_f32_16x16x32_bf16 v[192:195], a[132:135], v[116:119], v[192:195]\n"
    "v_mfma_f32_16x16x32_bf16 v[180:183], a[136:139], v[34:37], v[180:183]\n"
    "ds_read_b128 a[168:171], v16 offset:20480\n"
    "v_mfma_f32_16x16x32_bf16 v[196:199], a[136:139], v[116:119], v[196:199]\n"
    "s_waitcnt lgkmcnt(3)\n"
    "v_mfma_f32_16x16x32_bf16 v[172:175], a[144:147], v[38:41], v[172:175]\n"
    "ds_read_b128 a[128:131], v16 offset:21504\n"
    "v_mfma_f32_16x16x32_bf16 v[188:191], a[144:147], v[120:123], v[188:191]\n"
    "v_mfma_f32_16x16x32_bf16 v[176:179], a[148:151], v[38:41], v[176:179]\n"
    "ds_read_b128 a[132:135], v16 offset:22528\n"
    "v_mfma_f32_16x16x32_bf16 v[192:195], a[148:151], v[120:123], v[192:195]\n"
    "v_mfma_f32_16x16x32_bf16 v[180:183], a[152:155], v[38:41], v[180:183]\n"
    "ds_read_b128 a[136:139], v16 offset:23552\n"
    "v_mfma_f32_16x16x32_bf16 v[196:199], a[152:155], v[120:123], v[196:199]\n"
    "s_waitcnt lgkmcnt(3)\n"
    "v_mfma_f32_16x16x32_bf16 v[172:175], a[160:163], v[42:45], v[172:175]\n"
    "ds_read_b128 a[144:147], v16 offset:24576\n"
    "v_mfma_f32_16x16x32_bf16 v[188:191], a[160:163], v[124:127], v[188:191]\n"
    "v_mfma_f32_16x16x32_bf16 v[176:179], a[164:167], v[42:45], v[176:179]\n"
    "ds_read_b128 a[148:151], v16 offset:25600\n"
    "v_mfma_f32_16x16x32_bf16 v[192:195], a[164:167], v[124:127], v[192:195]\n"
    "v_mfma_f32_16x16x32_bf16 v[180:183], a[168:171], v[42:45], v[180:183]\n"
    "ds_read_b128 a[152:155], v16 offset:26624\n"
    "v_mfma_f32_16x16x32_bf16 v[196:199], a[168:171], v[124:127], v[196:199]\n"
    "s_waitcnt lgkmcnt(3)\n"
    "v_mfma_f32_16x16x32_bf16 v[172:175], a[128:131], v[46:49], v[172:175]\n"
    "ds_read_b128 a[160:163], v16 offset:27648\n"
    "v_mfma_f32_16x16x32_bf16 v[188:191], a[128:131], v[128:131], v[188:191]\n"
    "global_load_dword v54, v246, s[66:67] offset:-2048 sc1\n"
    "v_mfma_f32_16x16x32_bf16 v[176:179], a[132:135], v[46:49], v[176:179]\n"
    "ds_read_b128 a[164:167], v16 offset:28672\n"
    "global_load_dwordx4 v[6:9], v16, s[6:7] offset:-4096 sc1\n"
    "v_mfma_f32_16x16x32_bf16 v[192:195], a[132:135], v[128:131], v[192:195]\n"
    "v_mfma_f32_16x16x32_bf16 v[180:183], a[136:139], v[46:49], v[180:183]\n"
    "ds_read_b128 a[168:171], v16 offset:29696\n"
    "v_mfma_f32_16x16x32_bf16 v[196:199], a[136:139], v[128:131], v[196:199]\n"
    "s_waitcnt lgkmcnt(3)\n"
    "v_mfma_f32_16x16x32_bf16 v[172:175], a[144:147], v[50:53], v[172:175]\n"
    "ds_read_b128 a[128:131], v16 offset:30720\n"
    "global_load_dwordx4 v[100:103], v16, s[8:9] offset:-4096 sc1\n"
    "v_mfma_f32_16x16x32_bf16 v[188:191], a[144:147], v[132:135], v[188:191]\n"
    "v_mfma_f32_16x16x32_bf16 v[176:179], a[148:151], v[50:53], v[176:179]\n"
    "ds_read_b128 a[132:135], v16 offset:31744\n"
    "v_mfma_f32_16x16x32_bf16 v[192:195], a[148:151], v[132:135], v[192:195]\n"
    "v_mfma_f32_16x16x32_bf16 v[180:183], a[152:155], v[50:53], v[180:183]\n"
    "ds_read_b128 a[136:139], v16 offset:32768\n"
    "global_load_dword v55, v246, s[66:67] offset:-1792 sc1\n"
    "v_mfma_f32_16x16x32_bf16 v[196:199], a[152:155], v[132:135], v[196:199]\n"
    "s_waitcnt lgkmcnt(3)\n"
    "global_load_dwordx4 v[10:13], v16, s[6:7] offset:-3072 sc1\n"
    "v_mfma_f32_16x16x32_bf16 v[172:175], a[160:163], v[62:65], v[172:175]\n"
    "ds_read_b128 a[144:147], v16 offset:33792\n"
    "v_mfma_f32_16x16x32_bf16 v[188:191], a[160:163], v[136:139], v[188:191]\n"
    "v_mfma_f32_16x16x32_bf16 v[176:179], a[164:167], v[62:65], v[176:179]\n"
    "ds_read_b128 a[148:151], v16 offset:34816\n"
    "v_mfma_f32_16x16x32_bf16 v[192:195], a[164:167], v[136:139], v[192:195]\n"
    "global_load_dwordx4 v[104:107], v16, s[8:9] offset:-3072 sc1\n"
    "v_mfma_f32_16x16x32_bf16 v[180:183], a[168:171], v[62:65], v[180:183]\n"
    "ds_read_b128 a[152:155], v16 offset:35840\n"
    "v_mfma_f32_16x16x32_bf16 v[196:199], a[168:171], v[136:139], v[196:199]\n"
    "s_waitcnt lgkmcnt(3)\n"
    "v_mfma_f32_16x16x32_bf16 v[172:175], a[128:131], v[66:69], v[172:175]\n"
    "ds_read_b128 a[160:163], v16 offset:36864\n"
    "v_mfma_f32_16x16x32_bf16 v[188:191], a[128:131], v[140:143], v[188:191]\n"
    "global_load_dword v56, v246, s[66:67] offset:-1536 sc1\n"
    "v_mfma_f32_16x16x32_bf16 v[176:179], a[132:135], v[66:69], v[176:179]\n"
    "ds_read_b128 a[164:167], v16 offset:37888\n"
    "global_load_dwordx4 v[18:21], v16, s[6:7] offset:-2048 sc1\n"
    "v_mfma_f32_16x16x32_bf16 v[192:195], a[132:135], v[140:143], v[192:195]\n"
    "v_mfma_f32_16x16x32_bf16 v[180:183], a[136:139], v[66:69], v[180:183]\n"
    "ds_read_b128 a[168:171], v16 offset:38912\n"
    "v_mfma_f32_16x16x32_bf16 v[196:199], a[136:139], v[140:143], v[196:199]\n"
    "s_waitcnt lgkmcnt(3)\n"
    "v_mfma_f32_16x16x32_bf16 v[172:175], a[144:147], v[70:73], v[172:175]\n"
    "ds_read_b128 a[128:131], v16 offset:39936\n"
    "global_load_dwordx4 v[108:111], v16, s[8:9] offset:-2048 sc1\n"
    "v_mfma_f32_16x16x32_bf16 v[188:191], a[144:147], v[144:147], v[188:191]\n"
    "v_mfma_f32_16x16x32_bf16 v[176:179], a[148:151], v[70:73], v[176:179]\n"
    "ds_read_b128 a[132:135], v16 offset:40960\n"
    "v_mfma_f32_16x16x32_bf16 v[192:195], a[148:151], v[144:147], v[192:195]\n"
    "v_mfma_f32_16x16x32_bf16 v[180:183], a[152:155], v[70:73], v[180:183]\n"
    "ds_read_b128 a[136:139], v16 offset:41984\n"
    "global_load_dword v57, v246, s[66:67] offset:-1280 sc1\n"
    "v_mfma_f32_16x16x32_bf16 v[196:199], a[152:155], v[144:147], v[196:199]\n"
    "s_waitcnt lgkmcnt(3)\n"
    "global_load_dwordx4 v[30:33], v16, s[6:7] offset:-1024 sc1\n"
    "v_mfma_f32_16x16x32_bf16 v[172:175], a[160:163], v[78:81], v[172:175]\n"
    "ds_read_b128 a[144:147], v16 offset:43008\n"
    "v_mfma_f32_16x16x32_bf16 v[188:191], a[160:163], v[148:151], v[188:191]\n"
    "v_mfma_f32_16x16x32_bf16 v[176:179], a[164:167], v[78:81], v[176:179]\n"
    "ds_read_b128 a[148:151], v16 offset:44032\n"
    "v_mfma_f32_16x16x32_bf16 v[192:195], a[164:167], v[148:151], v[192:195]\n"
    "global_load_dwordx4 v[112:115], v16, s[8:9] offset:-1024 sc1\n"
    "v_mfma_f32_16x16x32_bf16 v[180:183], a[168:171], v[78:81], v[180:183]\n"
    "ds_read_b128 a[152:155], v16 offset:45056\n"
    "v_mfma_f32_16x16x32_bf16 v[196:199], a[168:171], v[148:151], v[196:199]\n"
    "s_waitcnt lgkmcnt(3)\n"
    "v_mfma_f32_16x16x32_bf16 v[172:175], a[128:131], v[88:91], v[172:175]\n"
    "ds_read_b128 a[160:163], v16 offset:46080\n"
    "v_mfma_f32_16x16x32_bf16 v[188:191], a[128:131], v[152:155], v[188:191]\n"
    "global_load_dword v58, v246, s[66:67] offset:-1024 sc1\n"
    "v_mfma_f32_16x16x32_bf16 v[176:179], a[132:135], v[88:91], v[176:179]\n"
    "ds_read_b128 a[164:167], v16 offset:47104\n"
    "global_load_dwordx4 v[34:37], v16, s[6:7] offset:0 sc1\n"
    "v_mfma_f32_16x16x32_bf16 v[192:195], a[132:135], v[152:155], v[192:195]\n"
    "v_mfma_f32_16x16x32_bf16 v[180:183], a[136:139], v[88:91], v[180:183]\n"
    "ds_read_b128 a[168:171], v16 offset:48128\n"
    "v_mfma_f32_16x16x32_bf16 v[196:199], a[136:139], v[152:155], v[196:199]\n"
    "s_waitcnt lgkmcnt(3)\n"
    "v_mfma_f32_16x16x32_bf16 v[172:175], a[144:147], v[92:95], v[172:175]\n"
    "ds_read_b64_tr_b16 a[128:129], v85 offset:0\n"
    "ds_read_b64_tr_b16 a[130:131], v85 offset:2048\n"
    "v_mfma_f32_16x16x32_bf16 v[188:191], a[144:147], v[156:159], v[188:191]\n"
    "ds_read_b64_tr_b16 a[132:133], v160 offset:0\n"
    "global_load_dwordx4 v[116:119], v16, s[8:9] offset:0 sc1\n"
    "v_mfma_f32_16x16x32_bf16 v[176:179], a[148:151], v[92:95], v[176:179]\n"
    "ds_read_b64_tr_b16 a[134:135], v160 offset:2048\n"
    "v_mfma_f32_16x16x32_bf16 v[192:195], a[148:151], v[156:159], v[192:195]\n"
    "ds_read_b64_tr_b16 a[136:137], v161 offset:0\n"
    "ds_read_b64_tr_b16 a[138:139], v161 offset:2048\n"
    "v_mfma_f32_16x16x32_bf16 v[180:183], a[152:155], v[92:95], v[180:183]\n"
    "ds_read_b64_tr_b16 a[140:141], v162 offset:0\n"
    "global_load_dword v59, v246, s[66:67] offset:-768 sc1\n"
    "v_mfma_f32_16x16x32_bf16 v[196:199], a[152:155], v[156:159], v[196:199]\n"
    "s_waitcnt lgkmcnt(7)\n"
    "ds_read_b64_tr_b16 a[142:143], v162 offset:2048\n"
    "v_mfma_f32_16x16x32_bf16 v[172:175], a[160:163], v[96:99], v[172:175]\n"
    "ds_read_b64_tr_b16 a[144:145], v85 offset:8192\n"
    "ds_read_b64_tr_b16 a[146:147], v85 offset:10240\n"
    "v_mfma_f32_16x16x32_bf16 v[188:191], a[160:163], v[164:167], v[188:191]\n"
    "ds_read_b64_tr_b16 a[148:149], v160 offset:8192\n"
    "global_load_dwordx4 v[38:41], v16, s[6:7] offset:1024 sc1\n"
    "v_mfma_f32_16x16x32_bf16 v[176:179], a[164:167], v[96:99], v[176:179]\n"
    "ds_read_b64_tr_b16 a[150:151], v160 offset:10240\n"
    "global_load_dwordx4 v[120:123], v16, s[8:9] offset:1024 sc1\n"
    "v_mfma_f32_16x16x32_bf16 v[192:195], a[164:167], v[164:167], v[192:195]\n"
    "ds_read_b64_tr_b16 a[152:153], v161 offset:8192\n"
    "ds_read_b64_tr_b16 a[154:155], v161 offset:10240\n"
    "v_mfma_f32_16x16x32_bf16 v[180:183], a[168:171], v[96:99], v[180:183]\n"
    "ds_read_b64_tr_b16 a[156:157], v162 offset:8192\n"
    "global_load_dword v60, v246, s[66:67] offset:-512 sc1\n"
    "v_mfma_f32_16x16x32_bf16 v[196:199], a[168:171], v[164:167], v[196:199]\n"
    "ds_read_b64_tr_b16 a[158:159], v162 offset:10240\n"
    "global_load_dwordx4 v[42:45], v16, s[6:7] offset:2048 sc1\n"
    "global_load_dwordx4 v[124:127], v16, s[8:9] offset:2048 sc1\n"
    "v_mov_b32 v216, 0x409d30fc\n"
    "s_nop 0\n"
    "v_permlane32_swap_b32 v180, v182\n"
    "v_permlane32_swap_b32 v181, v183\n"
    "v_pk_fma_f32 v[204:205], v[172:173], v[172:173], v[216:217] op_sel_hi:[1,1,0]\n"
    "v_pk_fma_f32 v[208:209], v[174:175], v[174:175], v[216:217] op_sel_hi:[1,1,0]\n"
    "v_pk_fma_f32 v[212:213], v[180:181], v[180:181], v[216:217] op_sel_hi:[1,1,0]\n"
    "v_pk_mul_f32 v[204:205], v[204:205], v[172:173]\n"
    "v_pk_mul_f32 v[208:209], v[208:209], v[174:175]\n"
    "v_pk_mul_f32 v[212:213], v[212:213], v[180:181]\n"
    "v_exp_f32_e64 v204, -v204\n"
    "v_exp_f32_e64 v205, -v205\n"
    "v_exp_f32_e64 v208, -v208\n"
    "v_exp_f32_e64 v209, -v209\n"
    "v_exp_f32_e64 v212, -v212\n"
    "v_exp_f32_e64 v213, -v213\n"
    "v_mov_b32_e64 v218, s89\n"
    "v_pk_fma_f32 v[204:205], v[204:205], v[218:219], v[218:219] op_sel_hi:[1,0,0]\n"
    "v_pk_fma_f32 v[208:209], v[208:209], v[218:219], v[218:219] op_sel_hi:[1,0,0]\n"
    "v_pk_fma_f32 v[212:213], v[212:213], v[218:219], v[218:219] op_sel_hi:[1,0,0]\n"
    "v_rcp_f32 v204, v204\n"
    "v_rcp_f32 v205, v205\n"
    "v_rcp_f32 v208, v208\n"
    "v_rcp_f32 v209, v209\n"
    "v_rcp_f32 v212, v212\n"
    "v_rcp_f32 v213, v213\n"
    "v_pk_mul_f32 v[176:177], v[172:173], v[176:177]\n"
    "v_pk_mul_f32 v[178:179], v[174:175], v[178:179]\n"
    "v_pk_mul_f32 v[182:183], v[180:181], v[182:183]\n"
    "v_pk_mul_f32 v[204:205], v[204:205], v[176:177]\n"
    "v_pk_mul_f32 v[208:209], v[208:209], v[178:179]\n"
    "v_pk_mul_f32 v[212:213], v[212:213], v[182:183]\n"
    "v_cvt_pk_bf16_f32 v184, v204, v205\n"
    "v_cvt_pk_bf16_f32 v185, v208, v209\n"
    "v_cvt_pk_bf16_f32 v186, v212, v213\n"
    "v_and_b32 v184, s34, v184\n"
    "v_and_b32 v185, s34, v185\n"
    "v_and_b32_e64 v186, s36, v186\n"
    "v_add3_u32 v187, v184, v185, v186\n"
    "v_sub_u32_e64 v187, s92, v187\n"
    "v_permlane32_swap_b32 v196, v198\n"
    "v_permlane32_swap_b32 v197, v199\n"
    "v_pk_fma_f32 v[220:221], v[188:189], v[188:189], v[216:217] op_sel_hi:[1,1,0]\n"
    "v_pk_fma_f32 v[224:225], v[190:191], v[190:191], v[216:217] op_sel_hi:[1,1,0]\n"
    "v_pk_fma_f32 v[228:229], v[196:197], v[196:197], v[216:217] op_sel_hi:[1,1,0]\n"
    "v_pk_mul_f32 v[220:221], v[220:221], v[188:189]\n"
    "v_pk_mul_f32 v[224:225], v[224:225], v[190:191]\n"
    "v_pk_mul_f32 v[228:229], v[228:229], v[196:197]\n"
    "v_exp_f32_e64 v220, -v220\n"
    "v_exp_f32_e64 v221, -v221\n"
    "v_exp_f32_e64 v224, -v224\n"
    "v_exp_f32_e64 v225, -v225\n"
    "v_exp_f32_e64 v228, -v228\n"
    "v_exp_f32_e64 v229, -v229\n"
    "v_pk_fma_f32 v[220:221], v[220:221], v[218:219], v[218:219] op_sel_hi:[1,0,0]\n"
    "v_pk_fma_f32 v[224:225], v[224:225], v[218:219], v[218:219] op_sel_hi:[1,0,0]\n"
    "v_pk_fma_f32 v[228:229], v[228:229], v[218:219], v[218:219] op_sel_hi:[1,0,0]\n"
    "v_rcp_f32 v220, v220\n"
    "v_rcp_f32 v221, v221\n"
    "v_rcp_f32 v224, v224\n"
    "v_rcp_f32 v225, v225\n"
    "v_rcp_f32 v228, v228\n"
    "v_rcp_f32 v229, v229\n"
    "v_pk_mul_f32 v[192:193], v[188:189], v[192:193]\n"
    "v_pk_mul_f32 v[194:195], v[190:191], v[194:195]\n"
    "v_pk_mul_f32 v[198:199], v[196:197], v[198:199]\n"
    "v_pk_mul_f32 v[220:221], v[220:221], v[192:193]\n"
    "v_pk_mul_f32 v[224:225], v[224:225], v[194:195]\n"
    "v_pk_mul_f32 v[228:229], v[228:229], v[198:199]\n"
    "v_cvt_pk_bf16_f32 v200, v220, v221\n"
    "v_cvt_pk_bf16_f32 v201, v224, v225\n"
    "v_cvt_pk_bf16_f32 v202, v228, v229\n"
    "v_and_b32 v200, s34, v200\n"
    "v_and_b32 v201, s34, v201\n"
    "v_and_b32_e64 v202, s36, v202\n"
    "v_add3_u32 v203, v200, v201, v202\n"
    "v_sub_u32_e64 v203, s92, v203\n"
    "global_store_dwordx4 v16, v[184:187], s[40:41] sc1\n"
    "global_store_dwordx4 v16, v[200:203], s[54:55] sc1\n"
    "global_load_dword v61, v246, s[66:67] offset:-256 sc1\n"
    "global_load_dwordx4 v[46:49], v16, s[6:7] offset:3072 sc1\n"
    ".Lpg3_%=:\n"
    "s_cbranch_execz .Lwarm_%=\n"
    "global_load_dwordx4 v[128:131], v16, s[8:9] offset:3072 sc1\n"
    "global_load_dword v236, v246, s[66:67] offset:2048 sc1\n"
    "global_load_dwordx4 v[50:53], v16, s[10:11] offset:-4096 sc1\n"
    "global_load_dwordx4 v[132:135], v16, s[12:13] offset:-4096 sc1\n"
    "s_waitcnt vmcnt(26)\n"
    "v_add3_u32 v3, v6, v8, s92\n"
    "v_add3_u32 v3, v100, v102, v3\n"
    "v_cmp_ne_u32_e64 vcc, v3, v54\n"
    "s_cbranch_vccnz .R74_%=\n"
    ".B75_%=:\n"
    "s_waitcnt lgkmcnt(14)\n"
    "v_mfma_f32_16x16x32_bf16 v[204:207], a[128:131], v[6:9], 0\n"
    "ds_read_b64_tr_b16 a[160:161], v85 offset:16384\n"
    "v_mfma_f32_16x16x32_bf16 v[220:223], a[128:131], v[100:103], 0\n"
    "ds_read_b64_tr_b16 a[162:163], v85 offset:18432\n"
    "s_waitcnt lgkmcnt(14)\n"
    "v_mfma_f32_16x16x32_bf16 v[208:211], a[132:135], v[6:9], 0\n"
    "s_waitcnt vmcnt(23)\n"
    "ds_read_b64_tr_b16 a[164:165], v160 offset:16384\n"
    "v_add3_u32 v3, v10, v12, s92\n"
    "v_mfma_f32_16x16x32_bf16 v[224:227], a[132:135], v[100:103], 0\n"
    "ds_read_b64_tr_b16 a[166:167], v160 offset:18432\n"
    "v_add3_u32 v3, v104, v106, v3\n"
    "s_waitcnt lgkmcnt(14)\n"
    "v_mfma_f32_16x16x32_bf16 v[212:215], a[136:139], v[6:9], 0\n"
    "ds_read_b64_tr_b16 a[168:169], v161 offset:16384\n"
    "v_cmp_ne_u32_e64 vcc, v3, v55\n"
    "v_mfma_f32_16x16x32_bf16 v[228:231], a[136:139], v[100:103], 0\n"
    "ds_read_b64_tr_b16 a[170:171], v161 offset:18432\n"
    "global_load_dword v237, v246, s[66:67] offset:2304 sc1\n"
    "s_waitcnt lgkmcnt(14)\n"
    "v_mfma_f32_16x16x32_bf16 v[216:219], a[140:143], v[6:9], 0\n"
    "s_cbranch_vccnz .R78_%=\n"
    ".B79_%=:\n"
    "ds_read_b64_tr_b16 a[172:173], v162 offset:16384\n"
    "v_mfma_f32_16x16x32_bf16 v[232:235], a[140:143], v[100:103], 0\n"
    "s_waitcnt lgkmcnt(7)\n"
    "ds_read_b64_tr_b16 a[174:175], v162 offset:18432\n"
    "global_load_dwordx4 v[62:65], v16, s[10:11] offset:-3072 sc1\n"
    "v_mfma_f32_16x16x32_bf16 v[204:207], a[144:147], v[10:13], v[204:207]\n"
    "s_waitcnt vmcnt(19)\n"
    "ds_read_b64_tr_b16 a[128:129], v85 offset:24576\n"
    "v_add3_u32 v3, v18, v20, s92\n"
    "v_add3_u32 v3, v108, v110, v3\n"
    "v_mfma_f32_16x16x32_bf16 v[220:223], a[144:147], v[104:107], v[220:223]\n"
    "s_barrier\n"
    "ds_read_b64_tr_b16 a[130:131], v85 offset:26624\n"
    "global_load_dwordx4 v[136:139], v16, s[12:13] offset:-3072 sc1\n"
    "v_add3_u32 v3, v30, v32, v3\n"
    "v_mfma_f32_16x16x32_bf16 v[208:211], a[148:151], v[10:13], v[208:211]\n"
    "ds_read_b64_tr_b16 a[132:133], v160 offset:24576\n"
    "v_add3_u32 v3, v112, v114, v3\n"
    "v_mfma_f32_16x16x32_bf16 v[224:227], a[148:151], v[104:107], v[224:227]\n"
    "ds_read_b64_tr_b16 a[134:135], v160 offset:26624\n"
    "v_add3_u32 v5, v56, v57, s93\n"
    "v_mfma_f32_16x16x32_bf16 v[212:215], a[152:155], v[10:13], v[212:215]\n"
    "ds_read_b64_tr_b16 a[136:137], v161 offset:24576\n"
    "v_cmp_ne_u32_e64 vcc, v3, v5\n"
    "v_mfma_f32_16x16x32_bf16 v[228:231], a[152:155], v[104:107], v[228:231]\n"
    "ds_read_b64_tr_b16 a[138:139], v161 offset:26624\n"
    "global_load_dword v238, v246, s[66:67] offset:2560 sc1\n"
    "v_mfma_f32_16x16x32_bf16 v[216:219], a[156:159], v[10:13], v[216:219]\n"
    "s_cbranch_vccnz .R82_%=\n"
    ".B83_%=:\n"
    "ds_read_b64_tr_b16 a[140:141], v162 offset:24576\n"
    "v_mfma_f32_16x16x32_bf16 v[232:235], a[156:159], v[104:107], v[232:235]\n"
    "s_waitcnt lgkmcnt(7)\n"
    "ds_read_b64_tr_b16 a[142:143], v162 offset:26624\n"
    "global_load_dwordx4 v[66:69], v16, s[10:11] offset:-2048 sc1\n"
    "v_mfma_f32_16x16x32_bf16 v[204:207], a[160:163], v[18:21], v[204:207]\n"
    "ds_read_b64_tr_b16 a[144:145], v74 offset:49152\n"
    "v_mfma_f32_16x16x32_bf16 v[220:223], a[160:163], v[108:111], v[220:223]\n"
    "ds_read_b64_tr_b16 a[146:147], v74 offset:51200\n"
    "global_load_dwordx4 v[140:143], v16, s[12:13] offset:-2048 sc1\n"
    "v_mfma_f32_16x16x32_bf16 v[208:211], a[164:167], v[18:21], v[208:211]\n"
    "ds_read_b64_tr_b16 a[148:149], v75 offset:49152\n"
    "v_mfma_f32_16x16x32_bf16 v[224:227], a[164:167], v[108:111], v[224:227]\n"
    "ds_read_b64_tr_b16 a[150:151], v75 offset:51200\n"
    "v_mfma_f32_16x16x32_bf16 v[212:215], a[168:171], v[18:21], v[212:215]\n"
    "ds_read_b64_tr_b16 a[152:153], v77 offset:49152\n"
    "global_load_dword v239, v246, s[66:67] offset:2816 sc1\n"
    "v_mfma_f32_16x16x32_bf16 v[228:231], a[168:171], v[108:111], v[228:231]\n"
    "ds_read_b64_tr_b16 a[154:155], v77 offset:51200\n"
    "global_load_dwordx4 v[70:73], v16, s[10:11] offset:-1024 sc1\n"
    "v_mfma_f32_16x16x32_bf16 v[216:219], a[172:175], v[18:21], v[216:219]\n"
    "ds_read_b64_tr_b16 a[156:157], v84 offset:49152\n"
    "v_mfma_f32_16x16x32_bf16 v[232:235], a[172:175], v[108:111], v[232:235]\n"
    "s_waitcnt lgkmcnt(7)\n"
    "ds_read_b64_tr_b16 a[158:159], v84 offset:51200\n"
    "v_mfma_f32_16x16x32_bf16 v[204:207], a[128:131], v[30:33], v[204:207]\n"
    "s_waitcnt vmcnt(19)\n"
    "ds_read_b64_tr_b16 a[160:161], v74 offset:57344\n"
    "v_add3_u32 v3, v34, v36, s92\n"
    "v_add3_u32 v3, v116, v118, v3\n"
    "v_mfma_f32_16x16x32_bf16 v[220:223], a[128:131], v[112:115], v[220:223]\n"
    "ds_read_b64_tr_b16 a[162:163], v74 offset:59392\n"
    "global_load_dwordx4 v[144:147], v16, s[12:13] offset:-1024 sc1\n"
    "v_add3_u32 v3, v38, v40, v3\n"
    "v_mfma_f32_16x16x32_bf16 v[208:211], a[132:135], v[30:33], v[208:211]\n"
    "ds_read_b64_tr_b16 a[164:165], v75 offset:57344\n"
    "v_add3_u32 v3, v120, v122, v3\n"
    "v_mfma_f32_16x16x32_bf16 v[224:227], a[132:135], v[112:115], v[224:227]\n"
    "ds_read_b64_tr_b16 a[166:167], v75 offset:59392\n"
    "v_add3_u32 v5, v58, v59, s93\n"
    "v_mfma_f32_16x16x32_bf16 v[212:215], a[136:139], v[30:33], v[212:215]\n"
    "ds_read_b64_tr_b16 a[168:169], v77 offset:57344\n"
    "v_cmp_ne_u32_e64 vcc, v3, v5\n"
    "v_mfma_f32_16x16x32_bf16 v[228:231], a[136:139], v[112:115], v[228:231]\n"
    "ds_read_b64_tr_b16 a[170:171], v77 offset:59392\n"
    "global_load_dword v240, v246, s[66:67] offset:3072 sc1\n"
    "v_mfma_f32_16x16x32_bf16 v[216:219], a[140:143], v[30:33], v[216:219]\n"
    "s_cbranch_vccnz .R86_%=\n"
    ".B87_%=:\n"
    "ds_read_b64_tr_b16 a[172:173], v84 offset:57344\n"
    "v_mfma_f32_16x16x32_bf16 v[232:235], a[140:143], v[112:115], v[232:235]\n"
    "s_waitcnt lgkmcnt(7)\n"
    "ds_read_b64_tr_b16 a[174:175], v84 offset:59392\n"
    "global_load_dwordx4 v[78:81], v16, s[10:11] offset:0 sc1\n"
    "v_mfma_f32_16x16x32_bf16 v[204:207], a[144:147], v[34:37], v[204:207]\n"
    "ds_read_b64_tr_b16 a[128:129], v82 offset:32768\n"
    "v_mfma_f32_16x16x32_bf16 v[220:223], a[144:147], v[116:119], v[220:223]\n"
    "ds_read_b64_tr_b16 a[130:131], v82 offset:34816\n"
    "global_load_dwordx4 v[148:151], v16, s[12:13] offset:0 sc1\n"
    "v_mfma_f32_16x16x32_bf16 v[208:211], a[148:151], v[34:37], v[208:211]\n"
    "ds_read_b64_tr_b16 a[132:133], v83 offset:32768\n"
    "v_mfma_f32_16x16x32_bf16 v[224:227], a[148:151], v[116:119], v[224:227]\n"
    "ds_read_b64_tr_b16 a[134:135], v83 offset:34816\n"
    "v_mfma_f32_16x16x32_bf16 v[212:215], a[152:155], v[34:37], v[212:215]\n"
    "ds_read_b64_tr_b16 a[136:137], v86 offset:32768\n"
    "global_load_dword v241, v246, s[66:67] offset:3328 sc1\n"
    "v_mfma_f32_16x16x32_bf16 v[228:231], a[152:155], v[116:119], v[228:231]\n"
    "ds_read_b64_tr_b16 a[138:139], v86 offset:34816\n"
    "global_load_dwordx4 v[88:91], v16, s[10:11] offset:1024 sc1\n"
    "v_mfma_f32_16x16x32_bf16 v[216:219], a[156:159], v[34:37], v[216:219]\n"
    "ds_read_b64_tr_b16 a[140:141], v87 offset:32768\n"
    "v_mfma_f32_16x16x32_bf16 v[232:235], a[156:159], v[116:119], v[232:235]\n"
    "s_waitcnt lgkmcnt(7)\n"
    "ds_read_b64_tr_b16 a[142:143], v87 offset:34816\n"
    "v_mfma_f32_16x16x32_bf16 v[204:207], a[160:163], v[38:41], v[204:207]\n"
    "s_waitcnt vmcnt(17)\n"
    "ds_read_b64_tr_b16 a[144:145], v82 offset:40960\n"
    "v_add3_u32 v3, v42, v44, s92\n"
    "v_add3_u32 v3, v124, v126, v3\n"
    "v_mfma_f32_16x16x32_bf16 v[220:223], a[160:163], v[120:123], v[220:223]\n"
    "ds_read_b64_tr_b16 a[146:147], v82 offset:43008\n"
    "global_load_dwordx4 v[152:155], v16, s[12:13] offset:1024 sc1\n"
    "v_add3_u32 v3, v46, v48, v3\n"
    "v_mfma_f32_16x16x32_bf16 v[208:211], a[164:167], v[38:41], v[208:211]\n"
    "ds_read_b64_tr_b16 a[148:149], v83 offset:40960\n"
    "v_add3_u32 v3, v128, v130, v3\n"
    "v_mfma_f32_16x16x32_bf16 v[224:227], a[164:167], v[120:123], v[224:227]\n"
    "ds_read_b64_tr_b16 a[150:151], v83 offset:43008\n"
    "v_add3_u32 v5, v60, v61, s93\n"
    "v_mfma_f32_16x16x32_bf16 v[212:215], a[168:171], v[38:41], v[212:215]\n"
    "ds_read_b64_tr_b16 a[152:153], v86 offset:40960\n"
    "v_cmp_ne_u32_e64 vcc, v3, v5\n"
    "v_mfma_f32_16x16x32_bf16 v[228:231], a[168:171], v[120:123], v[228:231]\n"
    "ds_read_b64_tr_b16 a[154:155], v86 offset:43008\n"
    "global_load_dword v242, v246, s[66:67] offset:3584 sc1\n"
    "v_mfma_f32_16x16x32_bf16 v[216:219], a[172:175], v[38:41], v[216:219]\n"
    "s_cbranch_vccnz .R90_%=\n"
    ".B91_%=:\n"
    "ds_read_b64_tr_b16 a[156:157], v87 offset:40960\n"
    "v_mfma_f32_16x16x32_bf16 v[232:235], a[172:175], v[120:123], v[232:235]\n"
    "s_waitcnt lgkmcnt(7)\n"
    "ds_read_b64_tr_b16 a[158:159], v87 offset:43008\n"
    "global_load_dwordx4 v[92:95], v16, s[10:11] offset:2048 sc1\n"
    "v_mfma_f32_16x16x32_bf16 v[204:207], a[128:131], v[42:45], v[204:207]\n"
    "ds_read_b64_tr_b16 a[160:161], v82 offset:49152\n"
    "v_mfma_f32_16x16x32_bf16 v[220:223], a[128:131], v[124:127], v[220:223]\n"
    "ds_read_b64_tr_b16 a[162:163], v82 offset:51200\n"
    "global_load_dwordx4 v[156:159], v16, s[12:13] offset:2048 sc1\n"
    "v_mfma_f32_16x16x32_bf16 v[208:211], a[132:135], v[42:45], v[208:211]\n"
    "ds_read_b64_tr_b16 a[164:165], v83 offset:49152\n"
    "v_mfma_f32_16x16x32_bf16 v[224:227], a[132:135], v[124:127], v[224:227]\n"
    "ds_read_b64_tr_b16 a[166:167], v83 offset:51200\n"
    "v_mfma_f32_16x16x32_bf16 v[212:215], a[136:139], v[42:45], v[212:215]\n"
    "ds_read_b64_tr_b16 a[168:169], v86 offset:49152\n"
    "global_load_dword v243, v246, s[66:67] offset:3840 sc1\n"
    "v_mfma_f32_16x16x32_bf16 v[228:231], a[136:139], v[124:127], v[228:231]\n"
    "ds_read_b64_tr_b16 a[170:171], v86 offset:51200\n"
    "global_load_dwordx4 v[96:99], v16, s[10:11] offset:3072 sc1\n"
    "v_mfma_f32_16x16x32_bf16 v[216:219], a[140:143], v[42:45], v[216:219]\n"
    "ds_read_b64_tr_b16 a[172:173], v87 offset:49152\n"
    "v_mfma_f32_16x16x32_bf16 v[232:235], a[140:143], v[124:127], v[232:235]\n"
    "s_waitcnt lgkmcnt(7)\n"
    "ds_read_b64_tr_b16 a[174:175], v87 offset:51200\n"
    "v_mfma_f32_16x16x32_bf16 v[204:207], a[144:147], v[46:49], v[204:207]\n"
    "s_waitcnt vmcnt(17)\n"
    "ds_read_b64_tr_b16 a[128:129], v82 offset:57344\n"
    "v_add3_u32 v3, v50, v52, s92\n"
    "v_add3_u32 v3, v132, v134, v3\n"
    "v_mfma_f32_16x16x32_bf16 v[220:223], a[144:147], v[128:131], v[220:223]\n"
    "s_barrier\n"
    "ds_read_b64_tr_b16 a[130:131], v82 offset:59392\n"
    "global_load_dwordx4 v[164:167], v16, s[12:13] offset:3072 sc1\n"
    "v_add3_u32 v3, v62, v64, v3\n"
    "v_mfma_f32_16x16x32_bf16 v[208:211], a[148:151], v[46:49], v[208:211]\n"
    "ds_read_b64_tr_b16 a[132:133], v83 offset:57344\n"
    "v_add3_u32 v3, v136, v138, v3\n"
    "v_mfma_f32_16x16x32_bf16 v[224:227], a[148:151], v[128:131], v[224:227]\n"
    "ds_read_b64_tr_b16 a[134:135], v83 offset:59392\n"
    "v_add3_u32 v5, v236, v237, s93\n"
    "v_mfma_f32_16x16x32_bf16 v[212:215], a[152:155], v[46:49], v[212:215]\n"
    "ds_read_b64_tr_b16 a[136:137], v86 offset:57344\n"
    "v_cmp_ne_u32 vcc, v3, v5\n"
    "v_mfma_f32_16x16x32_bf16 v[228:231], a[152:155], v[128:131], v[228:231]\n"
    "ds_read_b64_tr_b16 a[138:139], v86 offset:59392\n"
    "global_load_dwordx4 v[188:191], v26, s[52:53] offset:0 sc1\n"
    "v_mfma_f32_16x16x32_bf16 v[216:219], a[156:159], v[46:49], v[216:219]\n"
    "s_cbranch_vccnz .R94_%=\n"
    ".B95_%=:\n"
    "ds_read_b64_tr_b16 a[140:141], v87 offset:57344\n"
    "v_mfma_f32_16x16x32_bf16 v[232:235], a[156:159], v[128:131], v[232:235]\n"
    "s_waitcnt lgkmcnt(7)\n"
    "ds_read_b64_tr_b16 a[142:143], v87 offset:59392\n"
    "global_load_dwordx4 v[192:195], v26, s[52:53] offset:512 sc1\n"
    "v_mfma_f32_16x16x32_bf16 v[204:207], a[160:163], v[50:53], v[204:207]\n"
    "ds_read_b64_tr_b16 a[144:145], v82 offset:0\n"
    "v_mfma_f32_16x16x32_bf16 v[220:223], a[160:163], v[132:135], v[220:223]\n"
    "ds_read_b64_tr_b16 a[146:147], v82 offset:2048\n"
    "v_mfma_f32_16x16x32_bf16 v[208:211], a[164:167], v[50:53], v[208:211]\n"
    "ds_read_b64_tr_b16 a[148:149], v83 offset:0\n"
    "v_mfma_f32_16x16x32_bf16 v[224:227], a[164:167], v[132:135], v[224:227]\n"
    "ds_read_b64_tr_b16 a[150:151], v83 offset:2048\n"
    "v_mfma_f32_16x16x32_bf16 v[212:215], a[168:171], v[50:53], v[212:215]\n"
    "ds_read_b64_tr_b16 a[152:153], v86 offset:0\n"
    "v_mfma_f32_16x16x32_bf16 v[228:231], a[168:171], v[132:135], v[228:231]\n"
    "ds_read_b64_tr_b16 a[154:155], v86 offset:2048\n"
    "v_mfma_f32_16x16x32_bf16 v[216:219], a[172:175], v[50:53], v[216:219]\n"
    "ds_read_b64_tr_b16 a[156:157], v87 offset:0\n"
    "v_mfma_f32_16x16x32_bf16 v[232:235], a[172:175], v[132:135], v[232:235]\n"
    "s_waitcnt lgkmcnt(7)\n"
    "ds_read_b64_tr_b16 a[158:159], v87 offset:2048\n"
    "v_mfma_f32_16x16x32_bf16 v[204:207], a[128:131], v[62:65], v[204:207]\n"
    "s_waitcnt vmcnt(14)\n"
    "ds_read_b64_tr_b16 a[160:161], v82 offset:8192\n"
    "v_add3_u32 v3, v66, v68, s92\n"
    "v_add3_u32 v3, v140, v142, v3\n"
    "v_mfma_f32_16x16x32_bf16 v[220:223], a[128:131], v[136:139], v[220:223]\n"
    "ds_read_b64_tr_b16 a[162:163], v82 offset:10240\n"
    "v_add3_u32 v3, v70, v72, v3\n"
    "v_mfma_f32_16x16x32_bf16 v[208:211], a[132:135], v[62:65], v[208:211]\n"
    "ds_read_b64_tr_b16 a[164:165], v83 offset:8192\n"
    "v_add3_u32 v3, v144, v146, v3\n"
    "v_mfma_f32_16x16x32_bf16 v[224:227], a[132:135], v[136:139], v[224:227]\n"
    "ds_read_b64_tr_b16 a[166:167], v83 offset:10240\n"
    "global_load_dwordx4 v[6:9], v16, s[14:15] offset:-4096 sc1\n"
    "v_add3_u32 v5, v238, v239, s93\n"
    "v_mfma_f32_16x16x32_bf16 v[212:215], a[136:139], v[62:65], v[212:215]\n"
    "ds_read_b64_tr_b16 a[168:169], v86 offset:8192\n"
    "v_cmp_ne_u32_e64 vcc, v3, v5\n"
    "v_mfma_f32_16x16x32_bf16 v[228:231], a[136:139], v[136:139], v[228:231]\n"
    "ds_read_b64_tr_b16 a[170:171], v86 offset:10240\n"
    "global_load_dwordx4 v[100:103], v16, s[20:21] offset:-4096 sc1\n"
    "v_mfma_f32_16x16x32_bf16 v[216:219], a[140:143], v[62:65], v[216:219]\n"
    "s_cbranch_vccnz .R98_%=\n"
    ".B99_%=:\n"
    "ds_read_b64_tr_b16 a[172:173], v87 offset:8192\n"
    "v_mfma_f32_16x16x32_bf16 v[232:235], a[140:143], v[136:139], v[232:235]\n"
    "s_waitcnt lgkmcnt(7)\n"
    "ds_read_b64_tr_b16 a[174:175], v87 offset:10240\n"
    "v_mfma_f32_16x16x32_bf16 v[204:207], a[144:147], v[66:69], v[204:207]\n"
    "ds_read_b64_tr_b16 a[128:129], v82 offset:16384\n"
    "v_mfma_f32_16x16x32_bf16 v[220:223], a[144:147], v[140:143], v[220:223]\n"
    "ds_read_b64_tr_b16 a[130:131], v82 offset:18432\n"
    "global_load_dwordx4 v[10:13], v16, s[14:15] offset:-3072 sc1\n"
    "v_mfma_f32_16x16x32_bf16 v[208:211], a[148:151], v[66:69], v[208:211]\n"
    "ds_read_b64_tr_b16 a[132:133], v83 offset:16384\n"
    "v_mfma_f32_16x16x32_bf16 v[224:227], a[148:151], v[140:143], v[224:227]\n"
    "ds_read_b64_tr_b16 a[134:135], v83 offset:18432\n"
    "global_load_dwordx4 v[104:107], v16, s[20:21] offset:-3072 sc1\n"
    "v_mfma_f32_16x16x32_bf16 v[212:215], a[152:155], v[66:69], v[212:215]\n"
    "ds_read_b64_tr_b16 a[136:137], v86 offset:16384\n"
    "v_mfma_f32_16x16x32_bf16 v[228:231], a[152:155], v[140:143], v[228:231]\n"
    "ds_read_b64_tr_b16 a[138:139], v86 offset:18432\n"
    "v_mfma_f32_16x16x32_bf16 v[216:219], a[156:159], v[66:69], v[216:219]\n"
    "ds_read_b64_tr_b16 a[140:141], v87 offset:16384\n"
    "v_mfma_f32_16x16x32_bf16 v[232:235], a[156:159], v[140:143], v[232:235]\n"
    "s_waitcnt lgkmcnt(7)\n"
    "ds_read_b64_tr_b16 a[142:143], v87 offset:18432\n"
    "global_load_dwordx4 v[18:21], v16, s[14:15] offset:-2048 sc1\n"
    "v_mfma_f32_16x16x32_bf16 v[204:207], a[160:163], v[70:73], v[204:207]\n"
    "s_waitcnt vmcnt(13)\n"
    "ds_read_b64_tr_b16 a[144:145], v82 offset:24576\n"
    "v_add3_u32 v3, v78, v80, s92\n"
    ".Lpg4_%=:\n"
    "s_cbranch_execz .Lwarm_%=\n"
    "v_add3_u32 v3, v148, v150, v3\n"
    "v_mfma_f32_16x16x32_bf16 v[220:223], a[160:163], v[144:147], v[220:223]\n"
    "ds_read_b64_tr_b16 a[146:147], v82 offset:26624\n"
    "global_load_dwordx4 v[108:111], v16, s[20:21] offset:-2048 sc1\n"
    "v_add3_u32 v3, v88, v90, v3\n"
    "v_mfma_f32_16x16x32_bf16 v[208:211], a[164:167], v[70:73], v[208:211]\n"
    "ds_read_b64_tr_b16 a[148:149], v83 offset:24576\n"
    "v_add3_u32 v3, v152, v154, v3\n"
    "v_mfma_f32_16x16x32_bf16 v[224:227], a[164:167], v[144:147], v[224:227]\n"
    "ds_read_b64_tr_b16 a[150:151], v83 offset:26624\n"
    "v_add3_u32 v5, v240, v241, s93\n"
    "v_mfma_f32_16x16x32_bf16 v[212:215], a[168:171], v[70:73], v[212:215]\n"
    "ds_read_b64_tr_b16 a[152:153], v86 offset:24576\n"
    "v_cmp_ne_u32 vcc, v3, v5\n"
    "v_mfma_f32_16x16x32_bf16 v[228:231], a[168:171], v[144:147], v[228:231]\n"
    "ds_read_b64_tr_b16 a[154:155], v86 offset:26624\n"
    "global_load_dwordx4 v[30:33], v16, s[14:15] offset:-1024 sc1\n"
    "v_mfma_f32_16x16x32_bf16 v[216:219], a[172:175], v[70:73], v[216:219]\n"
    "s_cbranch_vccnz .R102_%=\n"
    ".B103_%=:\n"
    "ds_read_b64_tr_b16 a[156:157], v87 offset:24576\n"
    "v_mfma_f32_16x16x32_bf16 v[232:235], a[172:175], v[144:147], v[232:235]\n"
    "s_waitcnt lgkmcnt(7)\n"
    "ds_read_b64_tr_b16 a[158:159], v87 offset:26624\n"
    "v_mfma_f32_16x16x32_bf16 v[204:207], a[128:131], v[78:81], v[204:207]\n"
    "global_load_dwordx4 v[112:115], v16, s[20:21] offset:-1024 sc1\n"
    "v_mfma_f32_16x16x32_bf16 v[220:223], a[128:131], v[148:151], v[220:223]\n"
    "v_mfma_f32_16x16x32_bf16 v[208:211], a[132:135], v[78:81], v[208:211]\n"
    "v_mfma_f32_16x16x32_bf16 v[224:227], a[132:135], v[148:151], v[224:227]\n"
    "global_load_dwordx4 v[34:37], v16, s[14:15] offset:0 sc1\n"
    "v_mfma_f32_16x16x32_bf16 v[212:215], a[136:139], v[78:81], v[212:215]\n"
    "v_mfma_f32_16x16x32_bf16 v[228:231], a[136:139], v[148:151], v[228:231]\n"
    "global_load_dwordx4 v[116:119], v16, s[20:21] offset:0 sc1\n"
    "v_mfma_f32_16x16x32_bf16 v[216:219], a[140:143], v[78:81], v[216:219]\n"
    "v_mfma_f32_16x16x32_bf16 v[232:235], a[140:143], v[148:151], v[232:235]\n"
    "s_waitcnt lgkmcnt(0)\n"
    "v_mfma_f32_16x16x32_bf16 v[204:207], a[144:147], v[88:91], v[204:207]\n"
    "s_waitcnt vmcnt(12)\n"
    "v_add3_u32 v3, v92, v94, s92\n"
    "v_add3_u32 v3, v156, v158, v3\n"
    "v_mfma_f32_16x16x32_bf16 v[220:223], a[144:147], v[152:155], v[220:223]\n"
    "global_load_dwordx4 v[38:41], v16, s[14:15] offset:1024 sc1\n"
    "v_add3_u32 v3, v96, v98, v3\n"
    "v_mfma_f32_16x16x32_bf16 v[208:211], a[148:151], v[88:91], v[208:211]\n"
    "v_add3_u32 v3, v164, v166, v3\n"
    "s_waitcnt vmcnt(12)\n"
    "v_permlane16_swap_b32 v188, v190\n"
    "v_mfma_f32_16x16x32_bf16 v[224:227], a[148:151], v[152:155], v[224:227]\n"
    "global_load_dwordx4 v[120:123], v16, s[20:21] offset:1024 sc1\n"
    "v_add3_u32 v5, v242, v243, s93\n"
    "v_mfma_f32_16x16x32_bf16 v[212:215], a[152:155], v[88:91], v[212:215]\n"
    "v_cmp_ne_u32 vcc, v3, v5\n"
    "v_mfma_f32_16x16x32_bf16 v[228:231], a[152:155], v[152:155], v[228:231]\n"
    "v_permlane16_swap_b32 v189, v191\n"
    "v_lshlrev_b32 v172, 16, v188\n"
    "v_mfma_f32_16x16x32_bf16 v[216:219], a[156:159], v[88:91], v[216:219]\n"
    "s_cbranch_vccnz .R106_%=\n"
    ".B107_%=:\n"
    "v_mfma_f32_16x16x32_bf16 v[232:235], a[156:159], v[152:155], v[232:235]\n"
    "global_load_dwordx4 v[42:45], v16, s[14:15] offset:2048 sc1\n"
    "v_and_b32 v173, 0xffff0000, v188\n"
    "v_mfma_f32_16x16x32_bf16 v[204:207], a[0:3], v[92:95], v[204:207]\n"
    "v_lshlrev_b32_e64 v174, 16, v189\n"
    "v_and_b32 v175, 0xffff0000, v189\n"
    "v_mfma_f32_16x16x32_bf16 v[220:223], a[0:3], v[156:159], v[220:223]\n"
    "global_load_dwordx4 v[124:127], v16, s[20:21] offset:2048 sc1\n"
    "v_lshlrev_b32_e64 v176, 16, v190\n"
    "v_mfma_f32_16x16x32_bf16 v[208:211], a[4:7], v[92:95], v[208:211]\n"
    "v_and_b32 v177, 0xffff0000, v190\n"
    "v_lshlrev_b32_e64 v178, 16, v191\n"
    "v_mfma_f32_16x16x32_bf16 v[224:227], a[4:7], v[156:159], v[224:227]\n"
    "v_and_b32 v179, 0xffff0000, v191\n"
    "s_waitcnt vmcnt(14)\n"
    "v_permlane16_swap_b32 v192, v194\n"
    "v_mfma_f32_16x16x32_bf16 v[212:215], a[8:11], v[92:95], v[212:215]\n"
    "v_permlane16_swap_b32 v193, v195\n"
    "v_lshlrev_b32 v180, 16, v192\n"
    "v_mfma_f32_16x16x32_bf16 v[228:231], a[8:11], v[156:159], v[228:231]\n"
    "global_load_dwordx4 v[46:49], v16, s[14:15] offset:3072 sc1\n"
    "v_mfma_f32_16x16x32_bf16 v[216:219], a[12:15], v[92:95], v[216:219]\n"
    "v_and_b32 v181, 0xffff0000, v192\n"
    "v_lshlrev_b32 v182, 16, v193\n"
    "v_mfma_f32_16x16x32_bf16 v[232:235], a[12:15], v[156:159], v[232:235]\n"
    "global_load_dwordx4 v[128:131], v16, s[20:21] offset:3072 sc1\n"
    "v_and_b32 v183, 0xffff0000, v193\n"
    "v_mfma_f32_16x16x32_bf16 v[204:207], a[16:19], v[96:99], v[204:207]\n"
    "s_waitcnt vmcnt(12)\n"
    "v_add3_u32 v9, v9, v6, v7\n"
    "v_add3_u32 v9, v9, v8, s93\n"
    "v_add3_u32 v103, v103, v100, v101\n"
    "v_mfma_f32_16x16x32_bf16 v[220:223], a[16:19], v[164:167], v[220:223]\n"
    "v_add3_u32 v103, v103, v102, s93\n"
    "v_add3_u32 v13, v13, v10, v11\n"
    "v_mfma_f32_16x16x32_bf16 v[208:211], a[20:23], v[96:99], v[208:211]\n"
    "v_add3_u32 v13, v13, v12, s93\n"
    "v_add3_u32 v107, v107, v104, v105\n"
    "v_mfma_f32_16x16x32_bf16 v[224:227], a[20:23], v[164:167], v[224:227]\n"
    "v_add3_u32 v107, v107, v106, s93\n"
    "v_or_b32_e64 v3, v9, v103\n"
    "v_mfma_f32_16x16x32_bf16 v[212:215], a[24:27], v[96:99], v[212:215]\n"
    "v_or3_b32 v3, v3, v13, v107\n"
    "v_cmp_ne_u32_e64 vcc, 0, v3\n"
    "v_mfma_f32_16x16x32_bf16 v[228:231], a[24:27], v[164:167], v[228:231]\n"
    "v_lshlrev_b32_e64 v184, 16, v194\n"
    "v_mfma_f32_16x16x32_bf16 v[216:219], a[28:31], v[96:99], v[216:219]\n"
    "s_cbranch_vccnz .R110_%=\n"
    ".B111_%=:\n"
    "v_mov_b32 v13, v6\n"
    "v_mov_b32_e64 v107, v100\n"
    "v_mfma_f32_16x16x32_bf16 v[232:235], a[28:31], v[164:167], v[232:235]\n"
    "v_mfma_f32_16x16x32_bf16 v[204:207], a[32:35], v[10:13], v[204:207]\n"
    "v_and_b32 v185, 0xffff0000, v194\n"
    "v_mfma_f32_16x16x32_bf16 v[208:211], a[36:39], v[10:13], v[208:211]\n"
    "v_lshlrev_b32 v186, 16, v195\n"
    "v_and_b32 v187, 0xffff0000, v195\n"
    "v_mfma_f32_16x16x32_bf16 v[212:215], a[40:43], v[10:13], v[212:215]\n"
    "s_waitcnt vmcnt(10)\n"
    "v_add3_u32 v21, v21, v18, v19\n"
    "v_add3_u32 v21, v21, v20, s93\n"
    "v_mfma_f32_16x16x32_bf16 v[216:219], a[44:47], v[10:13], v[216:219]\n"
    "v_add3_u32 v111, v111, v108, v109\n"
    "v_add3_u32 v111, v111, v110, s93\n"
    "v_mfma_f32_16x16x32_bf16 v[220:223], a[32:35], v[104:107], v[220:223]\n"
    "v_or_b32 v3, v21, v111\n"
    "v_cmp_ne_u32 vcc, 0, v3\n"
    "v_add_f32 v204, v204, v172\n"
    "v_add_f32 v205, v205, v173\n"
    "v_mfma_f32_16x16x32_bf16 v[224:227], a[36:39], v[104:107], v[224:227]\n"
    "v_add_f32 v206, v206, v174\n"
    "v_add_f32 v208, v208, v176\n"
    "v_add_f32_e64 v209, v209, v177\n"
    "v_mfma_f32_16x16x32_bf16 v[228:231], a[40:43], v[104:107], v[228:231]\n"
    "s_cbranch_vccnz .R114_%=\n"
    ".B115_%=:\n"
    "v_mov_b32 v111, v101\n"
    "v_mov_b32 v21, v7\n"
    "v_add_f32 v207, v207, v175\n"
    "v_add_f32 v210, v210, v178\n"
    "v_add_f32 v212, v212, v180\n"
    "v_add_f32_e64 v213, v213, v181\n"
    "v_mfma_f32_16x16x32_bf16 v[232:235], a[44:47], v[104:107], v[232:235]\n"
    "v_add_f32 v211, v211, v179\n"
    "v_add_f32 v214, v214, v182\n"
    "v_add_f32 v216, v216, v184\n"
    "v_add_f32 v217, v217, v185\n"
    "v_mfma_f32_16x16x32_bf16 v[220:223], a[48:51], v[108:111], v[220:223]\n"
    "v_add_f32 v215, v215, v183\n"
    "v_add_f32 v218, v218, v186\n"
    "v_mfma_f32_16x16x32_bf16 v[224:227], a[52:55], v[108:111], v[224:227]\n"
    "v_add_f32 v219, v219, v187\n"
    "v_mfma_f32_16x16x32_bf16 v[228:231], a[56:59], v[108:111], v[228:231]\n"
    "s_waitcnt vmcnt(8)\n"
    "v_add3_u32 v33, v33, v30, v31\n"
    "v_add3_u32 v33, v33, v32, s93\n"
    "v_mfma_f32_16x16x32_bf16 v[232:235], a[60:63], v[108:111], v[232:235]\n"
    "v_add3_u32 v115, v115, v112, v113\n"
    "v_add3_u32 v115, v115, v114, s93\n"
    "v_mfma_f32_16x16x32_bf16 v[204:207], a[48:51], v[18:21], v[204:207]\n"
    "v_or_b32 v3, v33, v115\n"
    "v_cmp_ne_u32 vcc, 0, v3\n"
    "v_mfma_f32_16x16x32_bf16 v[208:211], a[52:55], v[18:21], v[208:211]\n"
    "v_mfma_f32_16x16x32_bf16 v[212:215], a[56:59], v[18:21], v[212:215]\n"
    "s_cbranch_vccnz .R118_%=\n"
    ".B119_%=:\n"
    "v_mov_b32 v33, v8\n"
    "v_mov_b32 v115, v102\n"
    "v_mfma_f32_16x16x32_bf16 v[216:219], a[60:63], v[18:21], v[216:219]\n"
    "v_mfma_f32_16x16x32_bf16 v[204:207], a[64:67], v[30:33], v[204:207]\n"
    "s_waitcnt vmcnt(4)\n"
    "v_add3_u32 v37, v37, v34, v35\n"
    "v_add3_u32 v37, v37, v36, s93\n"
    "v_add3_u32 v119, v119, v116, v117\n"
    "v_mfma_f32_16x16x32_bf16 v[220:223], a[64:67], v[112:115], v[220:223]\n"
    "v_add3_u32 v119, v119, v118, s93\n"
    "v_add3_u32 v41, v41, v38, v39\n"
    "v_mfma_f32_16x16x32_bf16 v[208:211], a[68:71], v[30:33], v[208:211]\n"
    "v_add3_u32 v41, v41, v40, s93\n"
    "v_add3_u32 v123, v123, v120, v121\n"
    "v_mfma_f32_16x16x32_bf16 v[224:227], a[68:71], v[112:115], v[224:227]\n"
    "v_add3_u32 v123, v123, v122, s93\n"
    "v_or_b32_e64 v3, v37, v119\n"
    "v_mfma_f32_16x16x32_bf16 v[212:215], a[72:75], v[30:33], v[212:215]\n"
    "v_or3_b32 v3, v3, v41, v123\n"
    "v_cmp_ne_u32_e64 vcc, 0, v3\n"
    "v_mfma_f32_16x16x32_bf16 v[228:231], a[72:75], v[112:115], v[228:231]\n"
    "v_mfma_f32_16x16x32_bf16 v[216:219], a[76:79], v[30:33], v[216:219]\n"
    "s_cbranch_vccnz .R122_%=\n"
    ".B123_%=:\n"
    "v_mov_b32 v41, v34\n"
    "v_mov_b32 v123, v116\n"
    "v_mfma_f32_16x16x32_bf16 v[232:235], a[76:79], v[112:115], v[232:235]\n"
    "v_mfma_f32_16x16x32_bf16 v[204:207], a[80:83], v[38:41], v[204:207]\n"
    "v_mfma_f32_16x16x32_bf16 v[220:223], a[80:83], v[120:123], v[220:223]\n"
    "v_mfma_f32_16x16x32_bf16 v[208:211], a[84:87], v[38:41], v[208:211]\n"
    "s_waitcnt vmcnt(2)\n"
    "v_add3_u32 v45, v45, v42, v43\n"
    "v_add3_u32 v45, v45, v44, s93\n"
    "v_mfma_f32_16x16x32_bf16 v[224:227], a[84:87], v[120:123], v[224:227]\n"
    "v_add3_u32 v127, v127, v124, v125\n"
    "v_add3_u32 v127, v127, v126, s93\n"
    "v_mfma_f32_16x16x32_bf16 v[212:215], a[88:91], v[38:41], v[212:215]\n"
    "v_or_b32 v3, v45, v127\n"
    "v_cmp_ne_u32 vcc, 0, v3\n"
    "v_mfma_f32_16x16x32_bf16 v[228:231], a[88:91], v[120:123], v[228:231]\n"
    "v_mfma_f32_16x16x32_bf16 v[216:219], a[92:95], v[38:41], v[216:219]\n"
    "s_cbranch_vccnz .R126_%=\n"
    ".B127_%=:\n"
    "v_mov_b32 v45, v35\n"
    "v_mov_b32 v127, v117\n"
    "v_mfma_f32_16x16x32_bf16 v[232:235], a[92:95], v[120:123], v[232:235]\n"
    "v_mfma_f32_16x16x32_bf16 v[204:207], a[96:99], v[42:45], v[204:207]\n"
    "v_mfma_f32_16x16x32_bf16 v[220:223], a[96:99], v[124:127], v[220:223]\n"
    "v_mfma_f32_16x16x32_bf16 v[208:211], a[100:103], v[42:45], v[208:211]\n"
    "s_waitcnt vmcnt(0)\n"
    "v_add3_u32 v49, v49, v46, v47\n"
    "v_add3_u32 v49, v49, v48, s93\n"
    "v_mfma_f32_16x16x32_bf16 v[224:227], a[100:103], v[124:127], v[224:227]\n"
    "v_add3_u32 v131, v131, v128, v129\n"
    "v_add3_u32 v131, v131, v130, s93\n"
    "v_mfma_f32_16x16x32_bf16 v[212:215], a[104:107], v[42:45], v[212:215]\n"
    "v_or_b32 v3, v49, v131\n"
    "v_cmp_ne_u32 vcc, 0, v3\n"
    "v_mfma_f32_16x16x32_bf16 v[228:231], a[104:107], v[124:127], v[228:231]\n"
    "v_mfma_f32_16x16x32_bf16 v[216:219], a[108:111], v[42:45], v[216:219]\n"
    "s_cbranch_vccnz .R130_%=\n"
    ".B131_%=:\n"
    "v_mov_b32 v131, v118\n"
    "v_mov_b32 v49, v36\n"
    "v_mfma_f32_16x16x32_bf16 v[232:235], a[108:111], v[124:127], v[232:235]\n"
    "v_mfma_f32_16x16x32_bf16 v[220:223], a[112:115], v[128:131], v[220:223]\n"
    "v_mfma_f32_16x16x32_bf16 v[224:227], a[116:119], v[128:131], v[224:227]\n"
    "v_mfma_f32_16x16x32_bf16 v[228:231], a[120:123], v[128:131], v[228:231]\n"
    "v_mfma_f32_16x16x32_bf16 v[232:235], a[124:127], v[128:131], v[232:235]\n"
    "v_mfma_f32_16x16x32_bf16 v[204:207], a[112:115], v[46:49], v[204:207]\n"
    "v_mfma_f32_16x16x32_bf16 v[208:211], a[116:119], v[46:49], v[208:211]\n"
    "v_mfma_f32_16x16x32_bf16 v[212:215], a[120:123], v[46:49], v[212:215]\n"
    "v_mfma_f32_16x16x32_bf16 v[216:219], a[124:127], v[46:49], v[216:219]\n"
    "s_nop 0\n"
    "ds_write_b128 v244, v[220:223] offset:0\n"
    "ds_write_b128 v244, v[224:227] offset:64\n"
    "ds_write_b128 v244, v[228:231] offset:128\n"
    "ds_write_b128 v244, v[232:235] offset:192\n"
    "ds_write_b128 v28, v[204:207] offset:0\n"
    "ds_write_b128 v28, v[208:211] offset:64\n"
    "ds_write_b128 v28, v[212:215] offset:128\n"
    "ds_write_b128 v28, v[216:219] offset:192\n"
    "s_waitcnt lgkmcnt(4)\n"
    "s_barrier\n"
    "ds_read_b128 v[100:103], v245 offset:4352\n"
    "ds_read_b128 v[104:107], v245 offset:4368\n"
    "ds_read_b128 v[10:13], v245 offset:16\n"
    "ds_read_b128 v[6:9], v245 offset:0\n"
    "ds_read_b128 v[112:115], v245 offset:6544\n"
    "ds_read_b128 v[30:33], v245 offset:2192\n"
    "ds_read_b128 v[108:111], v245 offset:6528\n"
    "ds_read_b128 v[18:21], v245 offset:2176\n"
    "s_waitcnt lgkmcnt(5)\n"
    "v_pk_add_f32 v[12:13], v[12:13], v[106:107]\n"
    "v_pk_add_f32 v[10:11], v[10:11], v[104:105]\n"
    "v_cvt_pk_bf16_f32 v191, v12, v13\n"
    "s_waitcnt lgkmcnt(4)\n"
    "v_pk_add_f32 v[6:7], v[6:7], v[100:101]\n"
    "v_pk_add_f32 v[8:9], v[8:9], v[102:103]\n"
    "v_cvt_pk_bf16_f32 v189, v8, v9\n"
    "v_cvt_pk_bf16_f32 v188, v6, v7\n"
    "v_cvt_pk_bf16_f32 v190, v10, v11\n"
    "s_waitcnt lgkmcnt(2)\n"
    "global_store_dwordx4 v27, v[188:191], s[4:5] offset:0 sc1\n"
    "v_pk_add_f32 v[32:33], v[32:33], v[114:115]\n"
    "v_pk_add_f32 v[30:31], v[30:31], v[112:113]\n"
    "v_cvt_pk_bf16_f32 v195, v32, v33\n"
    "v_cvt_pk_bf16_f32 v194, v30, v31\n"
    "s_waitcnt lgkmcnt(0)\n"
    "v_pk_add_f32 v[18:19], v[18:19], v[108:109]\n"
    "v_cvt_pk_bf16_f32 v192, v18, v19\n"
    "v_pk_add_f32 v[20:21], v[20:21], v[110:111]\n"
    "v_cvt_pk_bf16_f32 v193, v20, v21\n"
    "global_store_dwordx4 v27, v[192:195], s[4:5] offset:1024 sc1\n"
    "s_endpgm\n"
    ".Lend_%=:\n"
    ".R1_%=:\n"
    "s_nop 7\n"
    "s_mov_b32 s3, 0x30d40\n"
    ".Q4_%=:\n"
    "global_load_dwordx4 v[54:57], v84, s[20:21] offset:-2048 sc1\n"
    "global_load_dwordx4 v[58:61], v84, s[20:21] offset:2048 sc1\n"
    "s_waitcnt vmcnt(0)\n"
    "v_xor_b32 v62, s73, v54\n"
    "v_xor_b32 v63, s73, v58\n"
    "v_xor_b32 v64, s76, v55\n"
    "v_xor_b32 v65, s76, v59\n"
    "v_xor_b32 v66, s77, v56\n"
    "v_xor_b32 v67, s77, v60\n"
    "v_xor_b32 v68, s78, v57\n"
    "v_xor_b32_e64 v69, s78, v61\n"
    "v_or3_b32 v6, v62, v63, v64\n"
    "v_or3_b32 v7, v65, v66, v67\n"
    "v_or3_b32 v6, v6, v68, v69\n"
    "v_or_b32 v6, v6, v7\n"
    "v_and_b32 v6, v77, v6\n"
    "v_cmp_ne_u32 vcc, 0, v6\n"
    "s_cbranch_vccz .E3_%=\n"
    "s_sub_u32 s3, s3, 1\n"
    "s_cmp_lg_u32 s3, 0\n"
    "s_cbranch_scc1 .Q4_%=\n"
    ".E3_%=:\n"
    "s_nop 1\n"
    "s_branch .B2_%=\n"
    ".R5_%=:\n"
    "s_nop 7\n"
    "s_mov_b32 s3, 0x30d40\n"
    ".Q8_%=:\n"
    "global_load_dwordx4 v[54:57], v84, s[20:21] offset:0 sc1\n"
    "global_load_dwordx4 v[58:61], v84, s[20:21] offset:256 sc1\n"
    "s_waitcnt vmcnt(0)\n"
    "v_xor_b32 v62, s73, v54\n"
    "v_xor_b32 v63, s73, v58\n"
    "v_xor_b32 v64, s76, v55\n"
    "v_xor_b32 v65, s76, v59\n"
    "v_xor_b32 v66, s77, v56\n"
    "v_xor_b32 v67, s77, v60\n"
    "v_xor_b32 v68, s78, v57\n"
    "v_xor_b32_e64 v69, s78, v61\n"
    "v_or3_b32 v6, v62, v63, v64\n"
    "v_or3_b32 v7, v65, v66, v67\n"
    "v_or3_b32 v6, v6, v68, v69\n"
    "v_or_b32 v6, v6, v7\n"
    "v_and_b32 v6, v77, v6\n"
    "v_cmp_ne_u32 vcc, 0, v6\n"
    "s_cbranch_vccz .E7_%=\n"
    "s_sub_u32 s3, s3, 1\n"
    "s_cmp_lg_u32 s3, 0\n"
    "s_cbranch_scc1 .Q8_%=\n"
    ".E7_%=:\n"
    "s_nop 1\n"
    "s_branch .B6_%=\n"
    ".R10_%=:\n"
    "s_nop 7\n"
    "s_mov_b32 s3, 0x30d40\n"
    ".Q13_%=:\n"
    "buffer_load_dwordx4 v[6:9], v2, s[84:87], s81 offen offset:0 sc1\n"
    "buffer_load_dwordx4 v[100:103], v17, s[84:87], s81 offen offset:0 sc1\n"
    "s_waitcnt vmcnt(0)\n"
    "v_max3_u32 v3, v6, v8, v100\n"
    "v_max_u32 v3, v3, v102\n"
    "v_cmp_eq_u32_e64 vcc, -1, v3\n"
    "s_cbranch_vccz .E12_%=\n"
    "s_sub_u32 s3, s3, 1\n"
    "s_cmp_lg_u32 s3, 0\n"
    "s_cbranch_scc1 .Q13_%=\n"
    ".E12_%=:\n"
    "buffer_load_dwordx4 v[10:13], v2, s[84:87], s81 offen offset:512 sc1\n"
    "buffer_load_dwordx4 v[104:107], v17, s[84:87], s81 offen offset:512 sc1\n"
    "buffer_load_dwordx4 v[18:21], v2, s[84:87], s81 offen offset:1024 sc1\n"
    "buffer_load_dwordx4 v[108:111], v17, s[84:87], s81 offen offset:1024 sc1\n"
    "buffer_load_dwordx4 v[30:33], v2, s[84:87], s81 offen offset:1536 sc1\n"
    "buffer_load_dwordx4 v[112:115], v17, s[84:87], s81 offen offset:1536 sc1\n"
    "buffer_load_dwordx4 v[34:37], v2, s[84:87], s82 offen offset:0 sc1\n"
    "buffer_load_dwordx4 v[116:119], v17, s[84:87], s82 offen offset:0 sc1\n"
    "s_nop 1\n"
    "s_branch .B11_%=\n"
    ".R14_%=:\n"
    "s_nop 7\n"
    "s_mov_b32 s3, 0x30d40\n"
    ".Q17_%=:\n"
    "buffer_load_dwordx4 v[10:13], v2, s[84:87], s81 offen offset:512 sc1\n"
    "buffer_load_dwordx4 v[104:107], v17, s[84:87], s81 offen offset:512 sc1\n"
    "s_waitcnt vmcnt(0)\n"
    "v_max3_u32 v3, v10, v12, v104\n"
    "v_max_u32 v3, v3, v106\n"
    "v_cmp_eq_u32 vcc, -1, v3\n"
    "s_cbranch_vccz .E16_%=\n"
    "s_sub_u32 s3, s3, 1\n"
    "s_cmp_lg_u32 s3, 0\n"
    "s_cbranch_scc1 .Q17_%=\n"
    ".E16_%=:\n"
    "buffer_load_dwordx4 v[18:21], v2, s[84:87], s81 offen offset:1024 sc1\n"
    "buffer_load_dwordx4 v[108:111], v17, s[84:87], s81 offen offset:1024 sc1\n"
    "buffer_load_dwordx4 v[30:33], v2, s[84:87], s81 offen offset:1536 sc1\n"
    "buffer_load_dwordx4 v[112:115], v17, s[84:87], s81 offen offset:1536 sc1\n"
    "buffer_load_dwordx4 v[34:37], v2, s[84:87], s82 offen offset:0 sc1\n"
    "buffer_load_dwordx4 v[116:119], v17, s[84:87], s82 offen offset:0 sc1\n"
    "buffer_load_dwordx4 v[38:41], v2, s[84:87], s82 offen offset:512 sc1\n"
    "buffer_load_dwordx4 v[120:123], v17, s[84:87], s82 offen offset:512 sc1\n"
    "buffer_load_dwordx4 v[42:45], v2, s[84:87], s82 offen offset:1024 sc1\n"
    "s_nop 1\n"
    "s_branch .B15_%=\n"
    ".R18_%=:\n"
    "s_nop 7\n"
    "s_mov_b32 s3, 0x30d40\n"
    ".Q21_%=:\n"
    "buffer_load_dwordx4 v[18:21], v2, s[84:87], s81 offen offset:1024 sc1\n"
    "buffer_load_dwordx4 v[108:111], v17, s[84:87], s81 offen offset:1024 sc1\n"
    "s_waitcnt vmcnt(0)\n"
    "v_max3_u32 v3, v18, v20, v108\n"
    "v_max_u32 v3, v3, v110\n"
    "v_cmp_eq_u32 vcc, -1, v3\n"
    "s_cbranch_vccz .E20_%=\n"
    "s_sub_u32 s3, s3, 1\n"
    "s_cmp_lg_u32 s3, 0\n"
    "s_cbranch_scc1 .Q21_%=\n"
    ".E20_%=:\n"
    "buffer_load_dwordx4 v[30:33], v2, s[84:87], s81 offen offset:1536 sc1\n"
    "buffer_load_dwordx4 v[112:115], v17, s[84:87], s81 offen offset:1536 sc1\n"
    "buffer_load_dwordx4 v[34:37], v2, s[84:87], s82 offen offset:0 sc1\n"
    "buffer_load_dwordx4 v[116:119], v17, s[84:87], s82 offen offset:0 sc1\n"
    "buffer_load_dwordx4 v[38:41], v2, s[84:87], s82 offen offset:512 sc1\n"
    "buffer_load_dwordx4 v[120:123], v17, s[84:87], s82 offen offset:512 sc1\n"
    "buffer_load_dwordx4 v[42:45], v2, s[84:87], s82 offen offset:1024 sc1\n"
    "buffer_load_dwordx4 v[124:127], v17, s[84:87], s82 offen offset:1024 sc1\n"
    "buffer_load_dwordx4 v[46:49], v2, s[84:87], s82 offen offset:1536 sc1\n"
    "s_nop 1\n"
    "s_branch .B19_%=\n"
    ".R22_%=:\n"
    "s_nop 7\n"
    "s_mov_b32 s3, 0x30d40\n"
    ".Q25_%=:\n"
    "buffer_load_dwordx4 v[30:33], v2, s[84:87], s81 offen offset:1536 sc1\n"
    "buffer_load_dwordx4 v[112:115], v17, s[84:87], s81 offen offset:1536 sc1\n"
    "s_waitcnt vmcnt(0)\n"
    "v_max3_u32 v3, v30, v32, v112\n"
    "v_max_u32 v3, v3, v114\n"
    "v_cmp_eq_u32 vcc, -1, v3\n"
    "s_cbranch_vccz .E24_%=\n"
    "s_sub_u32 s3, s3, 1\n"
    "s_cmp_lg_u32 s3, 0\n"
    "s_cbranch_scc1 .Q25_%=\n"
    ".E24_%=:\n"
    "buffer_load_dwordx4 v[34:37], v2, s[84:87], s82 offen offset:0 sc1\n"
    "buffer_load_dwordx4 v[116:119], v17, s[84:87], s82 offen offset:0 sc1\n"
    "buffer_load_dwordx4 v[38:41], v2, s[84:87], s82 offen offset:512 sc1\n"
    "buffer_load_dwordx4 v[120:123], v17, s[84:87], s82 offen offset:512 sc1\n"
    "buffer_load_dwordx4 v[42:45], v2, s[84:87], s82 offen offset:1024 sc1\n"
    "buffer_load_dwordx4 v[124:127], v17, s[84:87], s82 offen offset:1024 sc1\n"
    "buffer_load_dwordx4 v[46:49], v2, s[84:87], s82 offen offset:1536 sc1\n"
    "buffer_load_dwordx4 v[128:131], v17, s[84:87], s82 offen offset:1536 sc1\n"
    "buffer_load_dwordx4 v[50:53], v2, s[84:87], s83 offen offset:0 sc1\n"
    "s_nop 1\n"
    "s_branch .B23_%=\n"
    ".R26_%=:\n"
    "s_nop 7\n"
    "s_mov_b32 s3, 0x30d40\n"
    ".Q29_%=:\n"
    "buffer_load_dwordx4 v[34:37], v2, s[84:87], s82 offen offset:0 sc1\n"
    "buffer_load_dwordx4 v[116:119], v17, s[84:87], s82 offen offset:0 sc1\n"
    "s_waitcnt vmcnt(0)\n"
    "v_max3_u32 v3, v34, v36, v116\n"
    "v_max_u32 v3, v3, v118\n"
    "v_cmp_eq_u32 vcc, -1, v3\n"
    "s_cbranch_vccz .E28_%=\n"
    "s_sub_u32 s3, s3, 1\n"
    "s_cmp_lg_u32 s3, 0\n"
    "s_cbranch_scc1 .Q29_%=\n"
    ".E28_%=:\n"
    "buffer_load_dwordx4 v[38:41], v2, s[84:87], s82 offen offset:512 sc1\n"
    "buffer_load_dwordx4 v[120:123], v17, s[84:87], s82 offen offset:512 sc1\n"
    "buffer_load_dwordx4 v[42:45], v2, s[84:87], s82 offen offset:1024 sc1\n"
    "buffer_load_dwordx4 v[124:127], v17, s[84:87], s82 offen offset:1024 sc1\n"
    "buffer_load_dwordx4 v[46:49], v2, s[84:87], s82 offen offset:1536 sc1\n"
    "buffer_load_dwordx4 v[128:131], v17, s[84:87], s82 offen offset:1536 sc1\n"
    "buffer_load_dwordx4 v[50:53], v2, s[84:87], s83 offen offset:0 sc1\n"
    "buffer_load_dwordx4 v[132:135], v17, s[84:87], s83 offen offset:0 sc1\n"
    "global_load_dword v1, v0, s[18:19]\n"
    "s_nop 1\n"
    "s_branch .B27_%=\n"
    ".R30_%=:\n"
    "s_nop 7\n"
    "s_mov_b32 s3, 0x30d40\n"
    ".Q33_%=:\n"
    "buffer_load_dwordx4 v[38:41], v2, s[84:87], s82 offen offset:512 sc1\n"
    "buffer_load_dwordx4 v[120:123], v17, s[84:87], s82 offen offset:512 sc1\n"
    "s_waitcnt vmcnt(0)\n"
    "v_max3_u32 v3, v38, v40, v120\n"
    "v_max_u32 v3, v3, v122\n"
    "v_cmp_eq_u32 vcc, -1, v3\n"
    "s_cbranch_vccz .E32_%=\n"
    "s_sub_u32 s3, s3, 1\n"
    "s_cmp_lg_u32 s3, 0\n"
    "s_cbranch_scc1 .Q33_%=\n"
    ".E32_%=:\n"
    "buffer_load_dwordx4 v[42:45], v2, s[84:87], s82 offen offset:1024 sc1\n"
    "buffer_load_dwordx4 v[124:127], v17, s[84:87], s82 offen offset:1024 sc1\n"
    "buffer_load_dwordx4 v[46:49], v2, s[84:87], s82 offen offset:1536 sc1\n"
    "buffer_load_dwordx4 v[128:131], v17, s[84:87], s82 offen offset:1536 sc1\n"
    "buffer_load_dwordx4 v[50:53], v2, s[84:87], s83 offen offset:0 sc1\n"
    "buffer_load_dwordx4 v[132:135], v17, s[84:87], s83 offen offset:0 sc1\n"
    "global_load_dword v1, v0, s[18:19]\n"
    "global_load_dword v1, v0, s[18:19]\n"
    "s_nop 1\n"
    "s_branch .B31_%=\n"
    ".R34_%=:\n"
    "s_nop 7\n"
    "s_mov_b32 s3, 0x30d40\n"
    ".Q37_%=:\n"
    "buffer_load_dwordx4 v[42:45], v2, s[84:87], s82 offen offset:1024 sc1\n"
    "buffer_load_dwordx4 v[124:127], v17, s[84:87], s82 offen offset:1024 sc1\n"
    "s_waitcnt vmcnt(0)\n"
    "v_max3_u32 v3, v42, v44, v124\n"
    "v_max_u32 v3, v3, v126\n"
    "v_cmp_eq_u32 vcc, -1, v3\n"
    "s_cbranch_vccz .E36_%=\n"
    "s_sub_u32 s3, s3, 1\n"
    "s_cmp_lg_u32 s3, 0\n"
    "s_cbranch_scc1 .Q37_%=\n"
    ".E36_%=:\n"
    "buffer_load_dwordx4 v[46:49], v2, s[84:87], s82 offen offset:1536 sc1\n"
    "buffer_load_dwordx4 v[128:131], v17, s[84:87], s82 offen offset:1536 sc1\n"
    "buffer_load_dwordx4 v[50:53], v2, s[84:87], s83 offen offset:0 sc1\n"
    "buffer_load_dwordx4 v[132:135], v17, s[84:87], s83 offen offset:0 sc1\n"
    "global_load_dword v1, v0, s[18:19]\n"
    "global_load_dword v1, v0, s[18:19]\n"
    "buffer_load_dwordx4 v[62:65], v2, s[84:87], s83 offen offset:512 sc1\n"
    "buffer_load_dwordx4 v[136:139], v17, s[84:87], s83 offen offset:512 sc1\n"
    "buffer_load_dwordx4 v[66:69], v2, s[84:87], s83 offen offset:1024 sc1\n"
    "buffer_load_dwordx4 v[140:143], v17, s[84:87], s83 offen offset:1024 sc1\n"
    "s_nop 1\n"
    "s_branch .B35_%=\n"
    ".R38_%=:\n"
    "s_nop 7\n"
    "s_mov_b32 s3, 0x30d40\n"
    ".Q41_%=:\n"
    "buffer_load_dwordx4 v[46:49], v2, s[84:87], s82 offen offset:1536 sc1\n"
    "buffer_load_dwordx4 v[128:131], v17, s[84:87], s82 offen offset:1536 sc1\n"
    "s_waitcnt vmcnt(0)\n"
    "v_max3_u32 v3, v46, v48, v128\n"
    "v_max_u32 v3, v3, v130\n"
    "v_cmp_eq_u32 vcc, -1, v3\n"
    "s_cbranch_vccz .E40_%=\n"
    "s_sub_u32 s3, s3, 1\n"
    "s_cmp_lg_u32 s3, 0\n"
    "s_cbranch_scc1 .Q41_%=\n"
    ".E40_%=:\n"
    "buffer_load_dwordx4 v[50:53], v2, s[84:87], s83 offen offset:0 sc1\n"
    "buffer_load_dwordx4 v[132:135], v17, s[84:87], s83 offen offset:0 sc1\n"
    "global_load_dword v1, v0, s[18:19]\n"
    "global_load_dword v1, v0, s[18:19]\n"
    "buffer_load_dwordx4 v[62:65], v2, s[84:87], s83 offen offset:512 sc1\n"
    "buffer_load_dwordx4 v[136:139], v17, s[84:87], s83 offen offset:512 sc1\n"
    "buffer_load_dwordx4 v[66:69], v2, s[84:87], s83 offen offset:1024 sc1\n"
    "buffer_load_dwordx4 v[140:143], v17, s[84:87], s83 offen offset:1024 sc1\n"
    "buffer_load_dwordx4 v[70:73], v2, s[84:87], s83 offen offset:1536 sc1\n"
    "buffer_load_dwordx4 v[144:147], v17, s[84:87], s83 offen offset:1536 sc1\n"
    "buffer_load_dwordx4 v[78:81], v2, s[84:87], s88 offen offset:0 sc1\n"
    "buffer_load_dwordx4 v[148:151], v17, s[84:87], s88 offen offset:0 sc1\n"
    "s_nop 1\n"
    "s_branch .B39_%=\n"
    ".R42_%=:\n"
    "s_nop 7\n"
    "s_mov_b32 s3, 0x30d40\n"
    ".Q45_%=:\n"
    "buffer_load_dwordx4 v[50:53], v2, s[84:87], s83 offen offset:0 sc1\n"
    "buffer_load_dwordx4 v[132:135], v17, s[84:87], s83 offen offset:0 sc1\n"
    "s_waitcnt vmcnt(0)\n"
    "v_max3_u32 v3, v50, v52, v132\n"
    "v_max_u32 v3, v3, v134\n"
    "v_cmp_eq_u32 vcc, -1, v3\n"
    "s_cbranch_vccz .E44_%=\n"
    "s_sub_u32 s3, s3, 1\n"
    "s_cmp_lg_u32 s3, 0\n"
    "s_cbranch_scc1 .Q45_%=\n"
    ".E44_%=:\n"
    "buffer_load_dwordx4 v[62:65], v2, s[84:87], s83 offen offset:512 sc1\n"
    "buffer_load_dwordx4 v[136:139], v17, s[84:87], s83 offen offset:512 sc1\n"
    "buffer_load_dwordx4 v[66:69], v2, s[84:87], s83 offen offset:1024 sc1\n"
    "buffer_load_dwordx4 v[140:143], v17, s[84:87], s83 offen offset:1024 sc1\n"
    "buffer_load_dwordx4 v[70:73], v2, s[84:87], s83 offen offset:1536 sc1\n"
    "buffer_load_dwordx4 v[144:147], v17, s[84:87], s83 offen offset:1536 sc1\n"
    "buffer_load_dwordx4 v[78:81], v2, s[84:87], s88 offen offset:0 sc1\n"
    "buffer_load_dwordx4 v[148:151], v17, s[84:87], s88 offen offset:0 sc1\n"
    "buffer_load_dwordx4 v[88:91], v2, s[84:87], s88 offen offset:512 sc1\n"
    "s_nop 1\n"
    "s_branch .B43_%=\n"
    ".R46_%=:\n"
    "s_nop 7\n"
    "s_mov_b32 s3, 0x30d40\n"
    ".Q49_%=:\n"
    "buffer_load_dwordx4 v[62:65], v2, s[84:87], s83 offen offset:512 sc1\n"
    "buffer_load_dwordx4 v[136:139], v17, s[84:87], s83 offen offset:512 sc1\n"
    "s_waitcnt vmcnt(0)\n"
    "v_max3_u32 v3, v62, v64, v136\n"
    "v_max_u32 v3, v3, v138\n"
    "v_cmp_eq_u32 vcc, -1, v3\n"
    "s_cbranch_vccz .E48_%=\n"
    "s_sub_u32 s3, s3, 1\n"
    "s_cmp_lg_u32 s3, 0\n"
    "s_cbranch_scc1 .Q49_%=\n"
    ".E48_%=:\n"
    "buffer_load_dwordx4 v[66:69], v2, s[84:87], s83 offen offset:1024 sc1\n"
    "buffer_load_dwordx4 v[140:143], v17, s[84:87], s83 offen offset:1024 sc1\n"
    "buffer_load_dwordx4 v[70:73], v2, s[84:87], s83 offen offset:1536 sc1\n"
    "buffer_load_dwordx4 v[144:147], v17, s[84:87], s83 offen offset:1536 sc1\n"
    "buffer_load_dwordx4 v[78:81], v2, s[84:87], s88 offen offset:0 sc1\n"
    "buffer_load_dwordx4 v[148:151], v17, s[84:87], s88 offen offset:0 sc1\n"
    "buffer_load_dwordx4 v[88:91], v2, s[84:87], s88 offen offset:512 sc1\n"
    "buffer_load_dwordx4 v[152:155], v17, s[84:87], s88 offen offset:512 sc1\n"
    "buffer_load_dwordx4 v[92:95], v2, s[84:87], s88 offen offset:1024 sc1\n"
    "s_nop 1\n"
    "s_branch .B47_%=\n"
    ".R50_%=:\n"
    "s_nop 7\n"
    "s_mov_b32 s3, 0x30d40\n"
    ".Q53_%=:\n"
    "buffer_load_dwordx4 v[66:69], v2, s[84:87], s83 offen offset:1024 sc1\n"
    "buffer_load_dwordx4 v[140:143], v17, s[84:87], s83 offen offset:1024 sc1\n"
    "s_waitcnt vmcnt(0)\n"
    "v_max3_u32 v3, v66, v68, v140\n"
    "v_max_u32 v3, v3, v142\n"
    "v_cmp_eq_u32 vcc, -1, v3\n"
    "s_cbranch_vccz .E52_%=\n"
    "s_sub_u32 s3, s3, 1\n"
    "s_cmp_lg_u32 s3, 0\n"
    "s_cbranch_scc1 .Q53_%=\n"
    ".E52_%=:\n"
    "buffer_load_dwordx4 v[70:73], v2, s[84:87], s83 offen offset:1536 sc1\n"
    "buffer_load_dwordx4 v[144:147], v17, s[84:87], s83 offen offset:1536 sc1\n"
    "buffer_load_dwordx4 v[78:81], v2, s[84:87], s88 offen offset:0 sc1\n"
    "buffer_load_dwordx4 v[148:151], v17, s[84:87], s88 offen offset:0 sc1\n"
    "buffer_load_dwordx4 v[88:91], v2, s[84:87], s88 offen offset:512 sc1\n"
    "buffer_load_dwordx4 v[152:155], v17, s[84:87], s88 offen offset:512 sc1\n"
    "buffer_load_dwordx4 v[92:95], v2, s[84:87], s88 offen offset:1024 sc1\n"
    "buffer_load_dwordx4 v[156:159], v17, s[84:87], s88 offen offset:1024 sc1\n"
    "buffer_load_dwordx4 v[96:99], v2, s[84:87], s88 offen offset:1536 sc1\n"
    "s_nop 1\n"
    "s_branch .B51_%=\n"
    ".R54_%=:\n"
    "s_nop 7\n"
    "s_mov_b32 s3, 0x30d40\n"
    ".Q57_%=:\n"
    "buffer_load_dwordx4 v[70:73], v2, s[84:87], s83 offen offset:1536 sc1\n"
    "buffer_load_dwordx4 v[144:147], v17, s[84:87], s83 offen offset:1536 sc1\n"
    "s_waitcnt vmcnt(0)\n"
    "v_max3_u32 v3, v70, v72, v144\n"
    "v_max_u32 v3, v3, v146\n"
    "v_cmp_eq_u32 vcc, -1, v3\n"
    "s_cbranch_vccz .E56_%=\n"
    "s_sub_u32 s3, s3, 1\n"
    "s_cmp_lg_u32 s3, 0\n"
    "s_cbranch_scc1 .Q57_%=\n"
    ".E56_%=:\n"
    "buffer_load_dwordx4 v[78:81], v2, s[84:87], s88 offen offset:0 sc1\n"
    "buffer_load_dwordx4 v[148:151], v17, s[84:87], s88 offen offset:0 sc1\n"
    "buffer_load_dwordx4 v[88:91], v2, s[84:87], s88 offen offset:512 sc1\n"
    "buffer_load_dwordx4 v[152:155], v17, s[84:87], s88 offen offset:512 sc1\n"
    "buffer_load_dwordx4 v[92:95], v2, s[84:87], s88 offen offset:1024 sc1\n"
    "buffer_load_dwordx4 v[156:159], v17, s[84:87], s88 offen offset:1024 sc1\n"
    "buffer_load_dwordx4 v[96:99], v2, s[84:87], s88 offen offset:1536 sc1\n"
    "buffer_load_dwordx4 v[164:167], v17, s[84:87], s88 offen offset:1536 sc1\n"
    "global_load_dword v1, v0, s[18:19]\n"
    "s_nop 1\n"
    "s_branch .B55_%=\n"
    ".R58_%=:\n"
    "s_nop 7\n"
    "s_mov_b32 s3, 0x30d40\n"
    ".Q61_%=:\n"
    "buffer_load_dwordx4 v[78:81], v2, s[84:87], s88 offen offset:0 sc1\n"
    "buffer_load_dwordx4 v[148:151], v17, s[84:87], s88 offen offset:0 sc1\n"
    "s_waitcnt vmcnt(0)\n"
    "v_max3_u32 v3, v78, v80, v148\n"
    "v_max_u32 v3, v3, v150\n"
    "v_cmp_eq_u32 vcc, -1, v3\n"
    "s_cbranch_vccz .E60_%=\n"
    "s_sub_u32 s3, s3, 1\n"
    "s_cmp_lg_u32 s3, 0\n"
    "s_cbranch_scc1 .Q61_%=\n"
    ".E60_%=:\n"
    "buffer_load_dwordx4 v[88:91], v2, s[84:87], s88 offen offset:512 sc1\n"
    "buffer_load_dwordx4 v[152:155], v17, s[84:87], s88 offen offset:512 sc1\n"
    "buffer_load_dwordx4 v[92:95], v2, s[84:87], s88 offen offset:1024 sc1\n"
    "buffer_load_dwordx4 v[156:159], v17, s[84:87], s88 offen offset:1024 sc1\n"
    "buffer_load_dwordx4 v[96:99], v2, s[84:87], s88 offen offset:1536 sc1\n"
    "buffer_load_dwordx4 v[164:167], v17, s[84:87], s88 offen offset:1536 sc1\n"
    "global_load_dword v1, v0, s[18:19]\n"
    "global_load_dword v1, v0, s[18:19]\n"
    "global_load_dword v1, v0, s[18:19]\n"
    "s_nop 1\n"
    "s_branch .B59_%=\n"
    ".R62_%=:\n"
    "s_nop 7\n"
    "s_mov_b32 s3, 0x30d40\n"
    ".Q65_%=:\n"
    "buffer_load_dwordx4 v[88:91], v2, s[84:87], s88 offen offset:512 sc1\n"
    "buffer_load_dwordx4 v[152:155], v17, s[84:87], s88 offen offset:512 sc1\n"
    "s_waitcnt vmcnt(0)\n"
    "v_max3_u32 v3, v88, v90, v152\n"
    "v_max_u32 v3, v3, v154\n"
    "v_cmp_eq_u32 vcc, -1, v3\n"
    "s_cbranch_vccz .E64_%=\n"
    "s_sub_u32 s3, s3, 1\n"
    "s_cmp_lg_u32 s3, 0\n"
    "s_cbranch_scc1 .Q65_%=\n"
    ".E64_%=:\n"
    "buffer_load_dwordx4 v[92:95], v2, s[84:87], s88 offen offset:1024 sc1\n"
    "buffer_load_dwordx4 v[156:159], v17, s[84:87], s88 offen offset:1024 sc1\n"
    "buffer_load_dwordx4 v[96:99], v2, s[84:87], s88 offen offset:1536 sc1\n"
    "buffer_load_dwordx4 v[164:167], v17, s[84:87], s88 offen offset:1536 sc1\n"
    "global_load_dword v1, v0, s[18:19]\n"
    "global_load_dword v1, v0, s[18:19]\n"
    "global_load_dword v1, v0, s[18:19]\n"
    "global_load_dword v1, v0, s[18:19]\n"
    "s_nop 1\n"
    "s_branch .B63_%=\n"
    ".R66_%=:\n"
    "s_nop 7\n"
    "s_mov_b32 s3, 0x30d40\n"
    ".Q69_%=:\n"
    "buffer_load_dwordx4 v[92:95], v2, s[84:87], s88 offen offset:1024 sc1\n"
    "buffer_load_dwordx4 v[156:159], v17, s[84:87], s88 offen offset:1024 sc1\n"
    "s_waitcnt vmcnt(0)\n"
    "v_max3_u32 v3, v92, v94, v156\n"
    "v_max_u32 v3, v3, v158\n"
    "v_cmp_eq_u32 vcc, -1, v3\n"
    "s_cbranch_vccz .E68_%=\n"
    "s_sub_u32 s3, s3, 1\n"
    "s_cmp_lg_u32 s3, 0\n"
    "s_cbranch_scc1 .Q69_%=\n"
    ".E68_%=:\n"
    "buffer_load_dwordx4 v[96:99], v2, s[84:87], s88 offen offset:1536 sc1\n"
    "buffer_load_dwordx4 v[164:167], v17, s[84:87], s88 offen offset:1536 sc1\n"
    "global_load_dword v1, v0, s[18:19]\n"
    "global_load_dword v1, v0, s[18:19]\n"
    "global_load_dword v1, v0, s[18:19]\n"
    "global_load_dword v1, v0, s[18:19]\n"
    "global_load_dword v1, v0, s[18:19]\n"
    "global_load_dword v1, v0, s[18:19]\n"
    "s_nop 1\n"
    "s_branch .B67_%=\n"
    ".R70_%=:\n"
    "s_nop 7\n"
    "s_mov_b32 s3, 0x30d40\n"
    ".Q73_%=:\n"
    "buffer_load_dwordx4 v[96:99], v2, s[84:87], s88 offen offset:1536 sc1\n"
    "buffer_load_dwordx4 v[164:167], v17, s[84:87], s88 offen offset:1536 sc1\n"
    "s_waitcnt vmcnt(0)\n"
    "v_max3_u32 v3, v96, v98, v164\n"
    "v_max_u32_e64 v3, v3, v166\n"
    "v_cmp_eq_u32 vcc, -1, v3\n"
    "s_cbranch_vccz .E72_%=\n"
    "s_sub_u32 s3, s3, 1\n"
    "s_cmp_lg_u32 s3, 0\n"
    "s_cbranch_scc1 .Q73_%=\n"
    ".E72_%=:\n"
    "s_nop 1\n"
    "s_branch .B71_%=\n"
    ".R74_%=:\n"
    "s_nop 7\n"
    "s_mov_b32 s3, 0x30d40\n"
    ".Q77_%=:\n"
    "global_load_dword v54, v246, s[66:67] offset:-2048 sc1\n"
    "global_load_dwordx4 v[6:9], v16, s[6:7] offset:-4096 sc1\n"
    "global_load_dwordx4 v[100:103], v16, s[8:9] offset:-4096 sc1\n"
    "s_waitcnt vmcnt(0)\n"
    "v_add3_u32 v3, v6, v8, s92\n"
    "v_add3_u32 v3, v100, v102, v3\n"
    "v_cmp_ne_u32 vcc, v3, v54\n"
    "s_cbranch_vccz .E76_%=\n"
    "s_sub_u32 s3, s3, 1\n"
    "s_cmp_lg_u32 s3, 0\n"
    "s_cbranch_scc1 .Q77_%=\n"
    ".E76_%=:\n"
    "global_load_dword v55, v246, s[66:67] offset:-1792 sc1\n"
    "global_load_dwordx4 v[10:13], v16, s[6:7] offset:-3072 sc1\n"
    "global_load_dwordx4 v[104:107], v16, s[8:9] offset:-3072 sc1\n"
    "global_load_dword v56, v246, s[66:67] offset:-1536 sc1\n"
    "global_load_dwordx4 v[18:21], v16, s[6:7] offset:-2048 sc1\n"
    "global_load_dwordx4 v[108:111], v16, s[8:9] offset:-2048 sc1\n"
    "global_load_dword v57, v246, s[66:67] offset:-1280 sc1\n"
    "global_load_dwordx4 v[30:33], v16, s[6:7] offset:-1024 sc1\n"
    "global_load_dwordx4 v[112:115], v16, s[8:9] offset:-1024 sc1\n"
    "global_load_dword v58, v246, s[66:67] offset:-1024 sc1\n"
    "global_load_dwordx4 v[34:37], v16, s[6:7] offset:0 sc1\n"
    "global_load_dwordx4 v[116:119], v16, s[8:9] offset:0 sc1\n"
    "global_load_dword v59, v246, s[66:67] offset:-768 sc1\n"
    "global_load_dwordx4 v[38:41], v16, s[6:7] offset:1024 sc1\n"
    "global_load_dwordx4 v[120:123], v16, s[8:9] offset:1024 sc1\n"
    "global_load_dword v60, v246, s[66:67] offset:-512 sc1\n"
    "global_load_dwordx4 v[42:45], v16, s[6:7] offset:2048 sc1\n"
    "global_load_dwordx4 v[124:127], v16, s[8:9] offset:2048 sc1\n"
    "global_load_dword v1, v0, s[18:19]\n"
    "global_load_dword v1, v0, s[18:19]\n"
    "global_load_dword v61, v246, s[66:67] offset:-256 sc1\n"
    "global_load_dwordx4 v[46:49], v16, s[6:7] offset:3072 sc1\n"
    "global_load_dwordx4 v[128:131], v16, s[8:9] offset:3072 sc1\n"
    "global_load_dword v236, v246, s[66:67] offset:2048 sc1\n"
    "global_load_dwordx4 v[50:53], v16, s[10:11] offset:-4096 sc1\n"
    "global_load_dwordx4 v[132:135], v16, s[12:13] offset:-4096 sc1\n"
    "s_nop 1\n"
    "s_branch .B75_%=\n"
    ".R78_%=:\n"
    "s_nop 7\n"
    "s_mov_b32 s3, 0x30d40\n"
    ".Q81_%=:\n"
    "global_load_dword v55, v246, s[66:67] offset:-1792 sc1\n"
    "global_load_dwordx4 v[10:13], v16, s[6:7] offset:-3072 sc1\n"
    "global_load_dwordx4 v[104:107], v16, s[8:9] offset:-3072 sc1\n"
    "s_waitcnt vmcnt(0)\n"
    "v_add3_u32 v3, v10, v12, s92\n"
    "v_add3_u32 v3, v104, v106, v3\n"
    "v_cmp_ne_u32_e64 vcc, v3, v55\n"
    "s_cbranch_vccz .E80_%=\n"
    "s_sub_u32 s3, s3, 1\n"
    "s_cmp_lg_u32 s3, 0\n"
    "s_cbranch_scc1 .Q81_%=\n"
    ".E80_%=:\n"
    "global_load_dword v56, v246, s[66:67] offset:-1536 sc1\n"
    "global_load_dwordx4 v[18:21], v16, s[6:7] offset:-2048 sc1\n"
    "global_load_dwordx4 v[108:111], v16, s[8:9] offset:-2048 sc1\n"
    "global_load_dword v57, v246, s[66:67] offset:-1280 sc1\n"
    "global_load_dwordx4 v[30:33], v16, s[6:7] offset:-1024 sc1\n"
    "global_load_dwordx4 v[112:115], v16, s[8:9] offset:-1024 sc1\n"
    "global_load_dword v58, v246, s[66:67] offset:-1024 sc1\n"
    "global_load_dwordx4 v[34:37], v16, s[6:7] offset:0 sc1\n"
    "global_load_dwordx4 v[116:119], v16, s[8:9] offset:0 sc1\n"
    "global_load_dword v59, v246, s[66:67] offset:-768 sc1\n"
    "global_load_dwordx4 v[38:41], v16, s[6:7] offset:1024 sc1\n"
    "global_load_dwordx4 v[120:123], v16, s[8:9] offset:1024 sc1\n"
    "global_load_dword v60, v246, s[66:67] offset:-512 sc1\n"
    "global_load_dwordx4 v[42:45], v16, s[6:7] offset:2048 sc1\n"
    "global_load_dwordx4 v[124:127], v16, s[8:9] offset:2048 sc1\n"
    "global_load_dword v1, v0, s[18:19]\n"
    "global_load_dword v1, v0, s[18:19]\n"
    "global_load_dword v61, v246, s[66:67] offset:-256 sc1\n"
    "global_load_dwordx4 v[46:49], v16, s[6:7] offset:3072 sc1\n"
    "global_load_dwordx4 v[128:131], v16, s[8:9] offset:3072 sc1\n"
    "global_load_dword v236, v246, s[66:67] offset:2048 sc1\n"
    "global_load_dwordx4 v[50:53], v16, s[10:11] offset:-4096 sc1\n"
    "global_load_dwordx4 v[132:135], v16, s[12:13] offset:-4096 sc1\n"
    "global_load_dword v237, v246, s[66:67] offset:2304 sc1\n"
    "s_nop 1\n"
    "s_branch .B79_%=\n"
    ".R82_%=:\n"
    "s_nop 7\n"
    "s_mov_b32 s3, 0x30d40\n"
    ".Q85_%=:\n"
    "global_load_dword v56, v246, s[66:67] offset:-1536 sc1\n"
    "global_load_dwordx4 v[18:21], v16, s[6:7] offset:-2048 sc1\n"
    "global_load_dwordx4 v[108:111], v16, s[8:9] offset:-2048 sc1\n"
    "global_load_dword v57, v246, s[66:67] offset:-1280 sc1\n"
    "global_load_dwordx4 v[30:33], v16, s[6:7] offset:-1024 sc1\n"
    "global_load_dwordx4 v[112:115], v16, s[8:9] offset:-1024 sc1\n"
    "s_waitcnt vmcnt(0)\n"
    "v_add3_u32 v3, v18, v20, s92\n"
    "v_add3_u32 v3, v108, v110, v3\n"
    "v_add3_u32 v3, v30, v32, v3\n"
    "v_add3_u32 v3, v112, v114, v3\n"
    "v_add3_u32 v5, v56, v57, s93\n"
    "v_cmp_ne_u32_e64 vcc, v3, v5\n"
    "s_cbranch_vccz .E84_%=\n"
    "s_sub_u32 s3, s3, 1\n"
    "s_cmp_lg_u32 s3, 0\n"
    "s_cbranch_scc1 .Q85_%=\n"
    ".E84_%=:\n"
    "global_load_dword v58, v246, s[66:67] offset:-1024 sc1\n"
    "global_load_dwordx4 v[34:37], v16, s[6:7] offset:0 sc1\n"
    "global_load_dwordx4 v[116:119], v16, s[8:9] offset:0 sc1\n"
    "global_load_dword v59, v246, s[66:67] offset:-768 sc1\n"
    "global_load_dwordx4 v[38:41], v16, s[6:7] offset:1024 sc1\n"
    "global_load_dwordx4 v[120:123], v16, s[8:9] offset:1024 sc1\n"
    "global_load_dword v60, v246, s[66:67] offset:-512 sc1\n"
    "global_load_dwordx4 v[42:45], v16, s[6:7] offset:2048 sc1\n"
    "global_load_dwordx4 v[124:127], v16, s[8:9] offset:2048 sc1\n"
    "global_load_dword v1, v0, s[18:19]\n"
    "global_load_dword v1, v0, s[18:19]\n"
    "global_load_dword v61, v246, s[66:67] offset:-256 sc1\n"
    "global_load_dwordx4 v[46:49], v16, s[6:7] offset:3072 sc1\n"
    "global_load_dwordx4 v[128:131], v16, s[8:9] offset:3072 sc1\n"
    "global_load_dword v236, v246, s[66:67] offset:2048 sc1\n"
    "global_load_dwordx4 v[50:53], v16, s[10:11] offset:-4096 sc1\n"
    "global_load_dwordx4 v[132:135], v16, s[12:13] offset:-4096 sc1\n"
    "global_load_dword v237, v246, s[66:67] offset:2304 sc1\n"
    "global_load_dwordx4 v[62:65], v16, s[10:11] offset:-3072 sc1\n"
    "global_load_dwordx4 v[136:139], v16, s[12:13] offset:-3072 sc1\n"
    "global_load_dword v238, v246, s[66:67] offset:2560 sc1\n"
    "s_nop 1\n"
    "s_branch .B83_%=\n"
    ".R86_%=:\n"
    "s_nop 7\n"
    "s_mov_b32 s3, 0x30d40\n"
    ".Q89_%=:\n"
    "global_load_dword v58, v246, s[66:67] offset:-1024 sc1\n"
    "global_load_dwordx4 v[34:37], v16, s[6:7] offset:0 sc1\n"
    "global_load_dwordx4 v[116:119], v16, s[8:9] offset:0 sc1\n"
    "global_load_dword v59, v246, s[66:67] offset:-768 sc1\n"
    "global_load_dwordx4 v[38:41], v16, s[6:7] offset:1024 sc1\n"
    "global_load_dwordx4 v[120:123], v16, s[8:9] offset:1024 sc1\n"
    "s_waitcnt vmcnt(0)\n"
    "v_add3_u32 v3, v34, v36, s92\n"
    "v_add3_u32 v3, v116, v118, v3\n"
    "v_add3_u32 v3, v38, v40, v3\n"
    "v_add3_u32 v3, v120, v122, v3\n"
    "v_add3_u32 v5, v58, v59, s93\n"
    "v_cmp_ne_u32_e64 vcc, v3, v5\n"
    "s_cbranch_vccz .E88_%=\n"
    "s_sub_u32 s3, s3, 1\n"
    "s_cmp_lg_u32 s3, 0\n"
    "s_cbranch_scc1 .Q89_%=\n"
    ".E88_%=:\n"
    "global_load_dword v60, v246, s[66:67] offset:-512 sc1\n"
    "global_load_dwordx4 v[42:45], v16, s[6:7] offset:2048 sc1\n"
    "global_load_dwordx4 v[124:127], v16, s[8:9] offset:2048 sc1\n"
    "global_load_dword v1, v0, s[18:19]\n"
    "global_load_dword v1, v0, s[18:19]\n"
    "global_load_dword v61, v246, s[66:67] offset:-256 sc1\n"
    "global_load_dwordx4 v[46:49], v16, s[6:7] offset:3072 sc1\n"
    "global_load_dwordx4 v[128:131], v16, s[8:9] offset:3072 sc1\n"
    "global_load_dword v236, v246, s[66:67] offset:2048 sc1\n"
    "global_load_dwordx4 v[50:53], v16, s[10:11] offset:-4096 sc1\n"
    "global_load_dwordx4 v[132:135], v16, s[12:13] offset:-4096 sc1\n"
    "global_load_dword v237, v246, s[66:67] offset:2304 sc1\n"
    "global_load_dwordx4 v[62:65], v16, s[10:11] offset:-3072 sc1\n"
    "global_load_dwordx4 v[136:139], v16, s[12:13] offset:-3072 sc1\n"
    "global_load_dword v238, v246, s[66:67] offset:2560 sc1\n"
    "global_load_dwordx4 v[66:69], v16, s[10:11] offset:-2048 sc1\n"
    "global_load_dwordx4 v[140:143], v16, s[12:13] offset:-2048 sc1\n"
    "global_load_dword v239, v246, s[66:67] offset:2816 sc1\n"
    "global_load_dwordx4 v[70:73], v16, s[10:11] offset:-1024 sc1\n"
    "global_load_dwordx4 v[144:147], v16, s[12:13] offset:-1024 sc1\n"
    "global_load_dword v240, v246, s[66:67] offset:3072 sc1\n"
    "s_nop 1\n"
    "s_branch .B87_%=\n"
    ".R90_%=:\n"
    "s_nop 7\n"
    "s_mov_b32 s3, 0x30d40\n"
    ".Q93_%=:\n"
    "global_load_dword v60, v246, s[66:67] offset:-512 sc1\n"
    "global_load_dwordx4 v[42:45], v16, s[6:7] offset:2048 sc1\n"
    "global_load_dwordx4 v[124:127], v16, s[8:9] offset:2048 sc1\n"
    "global_load_dword v61, v246, s[66:67] offset:-256 sc1\n"
    "global_load_dwordx4 v[46:49], v16, s[6:7] offset:3072 sc1\n"
    "global_load_dwordx4 v[128:131], v16, s[8:9] offset:3072 sc1\n"
    "s_waitcnt vmcnt(0)\n"
    "v_add3_u32 v3, v42, v44, s92\n"
    "v_add3_u32 v3, v124, v126, v3\n"
    "v_add3_u32 v3, v46, v48, v3\n"
    "v_add3_u32 v3, v128, v130, v3\n"
    "v_add3_u32 v5, v60, v61, s93\n"
    "v_cmp_ne_u32_e64 vcc, v3, v5\n"
    "s_cbranch_vccz .E92_%=\n"
    "s_sub_u32 s3, s3, 1\n"
    "s_cmp_lg_u32 s3, 0\n"
    "s_cbranch_scc1 .Q93_%=\n"
    ".E92_%=:\n"
    "global_load_dword v236, v246, s[66:67] offset:2048 sc1\n"
    "global_load_dwordx4 v[50:53], v16, s[10:11] offset:-4096 sc1\n"
    "global_load_dwordx4 v[132:135], v16, s[12:13] offset:-4096 sc1\n"
    "global_load_dword v237, v246, s[66:67] offset:2304 sc1\n"
    "global_load_dwordx4 v[62:65], v16, s[10:11] offset:-3072 sc1\n"
    "global_load_dwordx4 v[136:139], v16, s[12:13] offset:-3072 sc1\n"
    "global_load_dword v238, v246, s[66:67] offset:2560 sc1\n"
    "global_load_dwordx4 v[66:69], v16, s[10:11] offset:-2048 sc1\n"
    "global_load_dwordx4 v[140:143], v16, s[12:13] offset:-2048 sc1\n"
    "global_load_dword v239, v246, s[66:67] offset:2816 sc1\n"
    "global_load_dwordx4 v[70:73], v16, s[10:11] offset:-1024 sc1\n"
    "global_load_dwordx4 v[144:147], v16, s[12:13] offset:-1024 sc1\n"
    "global_load_dword v240, v246, s[66:67] offset:3072 sc1\n"
    "global_load_dwordx4 v[78:81], v16, s[10:11] offset:0 sc1\n"
    "global_load_dwordx4 v[148:151], v16, s[12:13] offset:0 sc1\n"
    "global_load_dword v241, v246, s[66:67] offset:3328 sc1\n"
    "global_load_dwordx4 v[88:91], v16, s[10:11] offset:1024 sc1\n"
    "global_load_dwordx4 v[152:155], v16, s[12:13] offset:1024 sc1\n"
    "global_load_dword v242, v246, s[66:67] offset:3584 sc1\n"
    "s_nop 1\n"
    "s_branch .B91_%=\n"
    ".R94_%=:\n"
    "s_nop 7\n"
    "s_mov_b32 s3, 0x30d40\n"
    ".Q97_%=:\n"
    "global_load_dword v236, v246, s[66:67] offset:2048 sc1\n"
    "global_load_dwordx4 v[50:53], v16, s[10:11] offset:-4096 sc1\n"
    "global_load_dwordx4 v[132:135], v16, s[12:13] offset:-4096 sc1\n"
    "global_load_dword v237, v246, s[66:67] offset:2304 sc1\n"
    "global_load_dwordx4 v[62:65], v16, s[10:11] offset:-3072 sc1\n"
    "global_load_dwordx4 v[136:139], v16, s[12:13] offset:-3072 sc1\n"
    "s_waitcnt vmcnt(0)\n"
    "v_add3_u32 v3, v50, v52, s92\n"
    "v_add3_u32 v3, v132, v134, v3\n"
    "v_add3_u32 v3, v62, v64, v3\n"
    "v_add3_u32 v3, v136, v138, v3\n"
    "v_add3_u32 v5, v236, v237, s93\n"
    "v_cmp_ne_u32_e64 vcc, v3, v5\n"
    "s_cbranch_vccz .E96_%=\n"
    "s_sub_u32 s3, s3, 1\n"
    "s_cmp_lg_u32 s3, 0\n"
    "s_cbranch_scc1 .Q97_%=\n"
    ".E96_%=:\n"
    "global_load_dword v238, v246, s[66:67] offset:2560 sc1\n"
    "global_load_dwordx4 v[66:69], v16, s[10:11] offset:-2048 sc1\n"
    "global_load_dwordx4 v[140:143], v16, s[12:13] offset:-2048 sc1\n"
    "global_load_dword v239, v246, s[66:67] offset:2816 sc1\n"
    "global_load_dwordx4 v[70:73], v16, s[10:11] offset:-1024 sc1\n"
    "global_load_dwordx4 v[144:147], v16, s[12:13] offset:-1024 sc1\n"
    "global_load_dword v240, v246, s[66:67] offset:3072 sc1\n"
    "global_load_dwordx4 v[78:81], v16, s[10:11] offset:0 sc1\n"
    "global_load_dwordx4 v[148:151], v16, s[12:13] offset:0 sc1\n"
    "global_load_dword v241, v246, s[66:67] offset:3328 sc1\n"
    "global_load_dwordx4 v[88:91], v16, s[10:11] offset:1024 sc1\n"
    "global_load_dwordx4 v[152:155], v16, s[12:13] offset:1024 sc1\n"
    "global_load_dword v242, v246, s[66:67] offset:3584 sc1\n"
    "global_load_dwordx4 v[92:95], v16, s[10:11] offset:2048 sc1\n"
    "global_load_dwordx4 v[156:159], v16, s[12:13] offset:2048 sc1\n"
    "global_load_dword v243, v246, s[66:67] offset:3840 sc1\n"
    "global_load_dwordx4 v[96:99], v16, s[10:11] offset:3072 sc1\n"
    "global_load_dwordx4 v[164:167], v16, s[12:13] offset:3072 sc1\n"
    "global_load_dword v1, v0, s[18:19]\n"
    "s_nop 1\n"
    "s_branch .B95_%=\n"
    ".R98_%=:\n"
    "s_nop 7\n"
    "s_mov_b32 s3, 0x30d40\n"
    ".Q101_%=:\n"
    "global_load_dword v238, v246, s[66:67] offset:2560 sc1\n"
    "global_load_dwordx4 v[66:69], v16, s[10:11] offset:-2048 sc1\n"
    "global_load_dwordx4 v[140:143], v16, s[12:13] offset:-2048 sc1\n"
    "global_load_dword v239, v246, s[66:67] offset:2816 sc1\n"
    "global_load_dwordx4 v[70:73], v16, s[10:11] offset:-1024 sc1\n"
    "global_load_dwordx4 v[144:147], v16, s[12:13] offset:-1024 sc1\n"
    "s_waitcnt vmcnt(0)\n"
    "v_add3_u32 v3, v66, v68, s92\n"
    "v_add3_u32 v3, v140, v142, v3\n"
    "v_add3_u32 v3, v70, v72, v3\n"
    "v_add3_u32 v3, v144, v146, v3\n"
    "v_add3_u32 v5, v238, v239, s93\n"
    "v_cmp_ne_u32_e64 vcc, v3, v5\n"
    "s_cbranch_vccz .E100_%=\n"
    "s_sub_u32 s3, s3, 1\n"
    "s_cmp_lg_u32 s3, 0\n"
    "s_cbranch_scc1 .Q101_%=\n"
    ".E100_%=:\n"
    "global_load_dword v240, v246, s[66:67] offset:3072 sc1\n"
    "global_load_dwordx4 v[78:81], v16, s[10:11] offset:0 sc1\n"
    "global_load_dwordx4 v[148:151], v16, s[12:13] offset:0 sc1\n"
    "global_load_dword v241, v246, s[66:67] offset:3328 sc1\n"
    "global_load_dwordx4 v[88:91], v16, s[10:11] offset:1024 sc1\n"
    "global_load_dwordx4 v[152:155], v16, s[12:13] offset:1024 sc1\n"
    "global_load_dword v242, v246, s[66:67] offset:3584 sc1\n"
    "global_load_dwordx4 v[92:95], v16, s[10:11] offset:2048 sc1\n"
    "global_load_dwordx4 v[156:159], v16, s[12:13] offset:2048 sc1\n"
    "global_load_dword v243, v246, s[66:67] offset:3840 sc1\n"
    "global_load_dwordx4 v[96:99], v16, s[10:11] offset:3072 sc1\n"
    "global_load_dwordx4 v[164:167], v16, s[12:13] offset:3072 sc1\n"
    "global_load_dword v1, v0, s[18:19]\n"
    "global_load_dword v1, v0, s[18:19]\n"
    "global_load_dwordx4 v[6:9], v16, s[14:15] offset:-4096 sc1\n"
    "global_load_dwordx4 v[100:103], v16, s[20:21] offset:-4096 sc1\n"
    "s_nop 1\n"
    "s_branch .B99_%=\n"
    ".R102_%=:\n"
    "s_nop 7\n"
    "s_mov_b32 s3, 0x30d40\n"
    ".Q105_%=:\n"
    "global_load_dword v240, v246, s[66:67] offset:3072 sc1\n"
    "global_load_dwordx4 v[78:81], v16, s[10:11] offset:0 sc1\n"
    "global_load_dwordx4 v[148:151], v16, s[12:13] offset:0 sc1\n"
    "global_load_dword v241, v246, s[66:67] offset:3328 sc1\n"
    "global_load_dwordx4 v[88:91], v16, s[10:11] offset:1024 sc1\n"
    "global_load_dwordx4 v[152:155], v16, s[12:13] offset:1024 sc1\n"
    "s_waitcnt vmcnt(0)\n"
    "v_add3_u32 v3, v78, v80, s92\n"
    "v_add3_u32 v3, v148, v150, v3\n"
    "v_add3_u32 v3, v88, v90, v3\n"
    "v_add3_u32 v3, v152, v154, v3\n"
    "v_add3_u32 v5, v240, v241, s93\n"
    "v_cmp_ne_u32_e64 vcc, v3, v5\n"
    "s_cbranch_vccz .E104_%=\n"
    "s_sub_u32 s3, s3, 1\n"
    "s_cmp_lg_u32 s3, 0\n"
    "s_cbranch_scc1 .Q105_%=\n"
    ".E104_%=:\n"
    "global_load_dword v242, v246, s[66:67] offset:3584 sc1\n"
    "global_load_dwordx4 v[92:95], v16, s[10:11] offset:2048 sc1\n"
    "global_load_dwordx4 v[156:159], v16, s[12:13] offset:2048 sc1\n"
    "global_load_dword v243, v246, s[66:67] offset:3840 sc1\n"
    "global_load_dwordx4 v[96:99], v16, s[10:11] offset:3072 sc1\n"
    "global_load_dwordx4 v[164:167], v16, s[12:13] offset:3072 sc1\n"
    "global_load_dword v1, v0, s[18:19]\n"
    "global_load_dword v1, v0, s[18:19]\n"
    "global_load_dwordx4 v[6:9], v16, s[14:15] offset:-4096 sc1\n"
    "global_load_dwordx4 v[100:103], v16, s[20:21] offset:-4096 sc1\n"
    "global_load_dwordx4 v[10:13], v16, s[14:15] offset:-3072 sc1\n"
    "global_load_dwordx4 v[104:107], v16, s[20:21] offset:-3072 sc1\n"
    "global_load_dwordx4 v[18:21], v16, s[14:15] offset:-2048 sc1\n"
    "global_load_dwordx4 v[108:111], v16, s[20:21] offset:-2048 sc1\n"
    "global_load_dwordx4 v[30:33], v16, s[14:15] offset:-1024 sc1\n"
    "s_nop 1\n"
    "s_branch .B103_%=\n"
    ".R106_%=:\n"
    "s_nop 7\n"
    "s_mov_b32 s3, 0x30d40\n"
    ".Q109_%=:\n"
    "global_load_dword v242, v246, s[66:67] offset:3584 sc1\n"
    "global_load_dwordx4 v[92:95], v16, s[10:11] offset:2048 sc1\n"
    "global_load_dwordx4 v[156:159], v16, s[12:13] offset:2048 sc1\n"
    "global_load_dword v243, v246, s[66:67] offset:3840 sc1\n"
    "global_load_dwordx4 v[96:99], v16, s[10:11] offset:3072 sc1\n"
    "global_load_dwordx4 v[164:167], v16, s[12:13] offset:3072 sc1\n"
    "s_waitcnt vmcnt(0)\n"
    "v_add3_u32 v3, v92, v94, s92\n"
    "v_add3_u32 v3, v156, v158, v3\n"
    "v_add3_u32 v3, v96, v98, v3\n"
    "v_add3_u32 v3, v164, v166, v3\n"
    "v_add3_u32 v5, v242, v243, s93\n"
    "v_cmp_ne_u32_e64 vcc, v3, v5\n"
    "s_cbranch_vccz .E108_%=\n"
    "s_sub_u32 s3, s3, 1\n"
    "s_cmp_lg_u32 s3, 0\n"
    "s_cbranch_scc1 .Q109_%=\n"
    ".E108_%=:\n"
    "global_load_dwordx4 v[6:9], v16, s[14:15] offset:-4096 sc1\n"
    "global_load_dwordx4 v[100:103], v16, s[20:21] offset:-4096 sc1\n"
    "global_load_dwordx4 v[10:13], v16, s[14:15] offset:-3072 sc1\n"
    "global_load_dwordx4 v[104:107], v16, s[20:21] offset:-3072 sc1\n"
    "global_load_dwordx4 v[18:21], v16, s[14:15] offset:-2048 sc1\n"
    "global_load_dwordx4 v[108:111], v16, s[20:21] offset:-2048 sc1\n"
    "global_load_dwordx4 v[30:33], v16, s[14:15] offset:-1024 sc1\n"
    "global_load_dwordx4 v[112:115], v16, s[20:21] offset:-1024 sc1\n"
    "global_load_dwordx4 v[34:37], v16, s[14:15] offset:0 sc1\n"
    "global_load_dwordx4 v[116:119], v16, s[20:21] offset:0 sc1\n"
    "global_load_dwordx4 v[38:41], v16, s[14:15] offset:1024 sc1\n"
    "global_load_dwordx4 v[120:123], v16, s[20:21] offset:1024 sc1\n"
    "s_nop 1\n"
    "s_branch .B107_%=\n"
    ".R110_%=:\n"
    "s_nop 7\n"
    "s_mov_b32 s3, 0x30d40\n"
    ".Q113_%=:\n"
    "global_load_dwordx4 v[6:9], v16, s[14:15] offset:-4096 sc1\n"
    "global_load_dwordx4 v[100:103], v16, s[20:21] offset:-4096 sc1\n"
    "global_load_dwordx4 v[10:13], v16, s[14:15] offset:-3072 sc1\n"
    "global_load_dwordx4 v[104:107], v16, s[20:21] offset:-3072 sc1\n"
    "s_waitcnt vmcnt(0)\n"
    "v_add3_u32 v9, v9, v6, v7\n"
    "v_add3_u32 v9, v9, v8, s93\n"
    "v_add3_u32 v103, v103, v100, v101\n"
    "v_add3_u32 v103, v103, v102, s93\n"
    "v_add3_u32 v13, v13, v10, v11\n"
    "v_add3_u32 v13, v13, v12, s93\n"
    "v_add3_u32 v107, v107, v104, v105\n"
    "v_add3_u32 v107, v107, v106, s93\n"
    "v_or_b32_e64 v3, v9, v103\n"
    "v_or3_b32 v3, v3, v13, v107\n"
    "v_cmp_ne_u32_e64 vcc, 0, v3\n"
    "s_cbranch_vccz .E112_%=\n"
    "s_sub_u32 s3, s3, 1\n"
    "s_cmp_lg_u32 s3, 0\n"
    "s_cbranch_scc1 .Q113_%=\n"
    ".E112_%=:\n"
    "global_load_dwordx4 v[18:21], v16, s[14:15] offset:-2048 sc1\n"
    "global_load_dwordx4 v[108:111], v16, s[20:21] offset:-2048 sc1\n"
    "global_load_dwordx4 v[30:33], v16, s[14:15] offset:-1024 sc1\n"
    "global_load_dwordx4 v[112:115], v16, s[20:21] offset:-1024 sc1\n"
    "global_load_dwordx4 v[34:37], v16, s[14:15] offset:0 sc1\n"
    "global_load_dwordx4 v[116:119], v16, s[20:21] offset:0 sc1\n"
    "global_load_dwordx4 v[38:41], v16, s[14:15] offset:1024 sc1\n"
    "global_load_dwordx4 v[120:123], v16, s[20:21] offset:1024 sc1\n"
    "global_load_dwordx4 v[42:45], v16, s[14:15] offset:2048 sc1\n"
    "global_load_dwordx4 v[124:127], v16, s[20:21] offset:2048 sc1\n"
    "global_load_dwordx4 v[46:49], v16, s[14:15] offset:3072 sc1\n"
    "global_load_dwordx4 v[128:131], v16, s[20:21] offset:3072 sc1\n"
    "s_nop 1\n"
    "s_branch .B111_%=\n"
    ".R114_%=:\n"
    "s_nop 7\n"
    "s_mov_b32 s3, 0x30d40\n"
    ".Q117_%=:\n"
    "global_load_dwordx4 v[18:21], v16, s[14:15] offset:-2048 sc1\n"
    "global_load_dwordx4 v[108:111], v16, s[20:21] offset:-2048 sc1\n"
    "s_waitcnt vmcnt(0)\n"
    "v_add3_u32 v21, v21, v18, v19\n"
    "v_add3_u32 v21, v21, v20, s93\n"
    "v_add3_u32 v111, v111, v108, v109\n"
    "v_add3_u32 v111, v111, v110, s93\n"
    "v_or_b32 v3, v21, v111\n"
    "v_cmp_ne_u32 vcc, 0, v3\n"
    "s_cbranch_vccz .E116_%=\n"
    "s_sub_u32 s3, s3, 1\n"
    "s_cmp_lg_u32 s3, 0\n"
    "s_cbranch_scc1 .Q117_%=\n"
    ".E116_%=:\n"
    "global_load_dwordx4 v[30:33], v16, s[14:15] offset:-1024 sc1\n"
    "global_load_dwordx4 v[112:115], v16, s[20:21] offset:-1024 sc1\n"
    "global_load_dwordx4 v[34:37], v16, s[14:15] offset:0 sc1\n"
    "global_load_dwordx4 v[116:119], v16, s[20:21] offset:0 sc1\n"
    "global_load_dwordx4 v[38:41], v16, s[14:15] offset:1024 sc1\n"
    "global_load_dwordx4 v[120:123], v16, s[20:21] offset:1024 sc1\n"
    "global_load_dwordx4 v[42:45], v16, s[14:15] offset:2048 sc1\n"
    "global_load_dwordx4 v[124:127], v16, s[20:21] offset:2048 sc1\n"
    "global_load_dwordx4 v[46:49], v16, s[14:15] offset:3072 sc1\n"
    "global_load_dwordx4 v[128:131], v16, s[20:21] offset:3072 sc1\n"
    "s_nop 1\n"
    "s_branch .B115_%=\n"
    ".R118_%=:\n"
    "s_nop 7\n"
    "s_mov_b32 s3, 0x30d40\n"
    ".Q121_%=:\n"
    "global_load_dwordx4 v[30:33], v16, s[14:15] offset:-1024 sc1\n"
    "global_load_dwordx4 v[112:115], v16, s[20:21] offset:-1024 sc1\n"
    "s_waitcnt vmcnt(0)\n"
    "v_add3_u32 v33, v33, v30, v31\n"
    "v_add3_u32 v33, v33, v32, s93\n"
    "v_add3_u32 v115, v115, v112, v113\n"
    "v_add3_u32 v115, v115, v114, s93\n"
    "v_or_b32 v3, v33, v115\n"
    "v_cmp_ne_u32 vcc, 0, v3\n"
    "s_cbranch_vccz .E120_%=\n"
    "s_sub_u32 s3, s3, 1\n"
    "s_cmp_lg_u32 s3, 0\n"
    "s_cbranch_scc1 .Q121_%=\n"
    ".E120_%=:\n"
    "global_load_dwordx4 v[34:37], v16, s[14:15] offset:0 sc1\n"
    "global_load_dwordx4 v[116:119], v16, s[20:21] offset:0 sc1\n"
    "global_load_dwordx4 v[38:41], v16, s[14:15] offset:1024 sc1\n"
    "global_load_dwordx4 v[120:123], v16, s[20:21] offset:1024 sc1\n"
    "global_load_dwordx4 v[42:45], v16, s[14:15] offset:2048 sc1\n"
    "global_load_dwordx4 v[124:127], v16, s[20:21] offset:2048 sc1\n"
    "global_load_dwordx4 v[46:49], v16, s[14:15] offset:3072 sc1\n"
    "global_load_dwordx4 v[128:131], v16, s[20:21] offset:3072 sc1\n"
    "s_nop 1\n"
    "s_branch .B119_%=\n"
    ".R122_%=:\n"
    "s_nop 7\n"
    "s_mov_b32 s3, 0x30d40\n"
    ".Q125_%=:\n"
    "global_load_dwordx4 v[34:37], v16, s[14:15] offset:0 sc1\n"
    "global_load_dwordx4 v[116:119], v16, s[20:21] offset:0 sc1\n"
    "global_load_dwordx4 v[38:41], v16, s[14:15] offset:1024 sc1\n"
    "global_load_dwordx4 v[120:123], v16, s[20:21] offset:1024 sc1\n"
    "s_waitcnt vmcnt(0)\n"
    "v_add3_u32 v37, v37, v34, v35\n"
    "v_add3_u32 v37, v37, v36, s93\n"
    "v_add3_u32 v119, v119, v116, v117\n"
    "v_add3_u32 v119, v119, v118, s93\n"
    "v_add3_u32 v41, v41, v38, v39\n"
    "v_add3_u32 v41, v41, v40, s93\n"
    "v_add3_u32 v123, v123, v120, v121\n"
    "v_add3_u32 v123, v123, v122, s93\n"
    "v_or_b32_e64 v3, v37, v119\n"
    "v_or3_b32 v3, v3, v41, v123\n"
    "v_cmp_ne_u32_e64 vcc, 0, v3\n"
    "s_cbranch_vccz .E124_%=\n"
    "s_sub_u32 s3, s3, 1\n"
    "s_cmp_lg_u32 s3, 0\n"
    "s_cbranch_scc1 .Q125_%=\n"
    ".E124_%=:\n"
    "global_load_dwordx4 v[42:45], v16, s[14:15] offset:2048 sc1\n"
    "global_load_dwordx4 v[124:127], v16, s[20:21] offset:2048 sc1\n"
    "global_load_dwordx4 v[46:49], v16, s[14:15] offset:3072 sc1\n"
    "global_load_dwordx4 v[128:131], v16, s[20:21] offset:3072 sc1\n"
    "s_nop 1\n"
    "s_branch .B123_%=\n"
    ".R126_%=:\n"
    "s_nop 7\n"
    "s_mov_b32 s3, 0x30d40\n"
    ".Q129_%=:\n"
    "global_load_dwordx4 v[42:45], v16, s[14:15] offset:2048 sc1\n"
    "global_load_dwordx4 v[124:127], v16, s[20:21] offset:2048 sc1\n"
    "s_waitcnt vmcnt(0)\n"
    "v_add3_u32 v45, v45, v42, v43\n"
    "v_add3_u32 v45, v45, v44, s93\n"
    "v_add3_u32 v127, v127, v124, v125\n"
    "v_add3_u32 v127, v127, v126, s93\n"
    "v_or_b32 v3, v45, v127\n"
    "v_cmp_ne_u32 vcc, 0, v3\n"
    "s_cbranch_vccz .E128_%=\n"
    "s_sub_u32 s3, s3, 1\n"
    "s_cmp_lg_u32 s3, 0\n"
    "s_cbranch_scc1 .Q129_%=\n"
    ".E128_%=:\n"
    "global_load_dwordx4 v[46:49], v16, s[14:15] offset:3072 sc1\n"
    "global_load_dwordx4 v[128:131], v16, s[20:21] offset:3072 sc1\n"
    "s_nop 1\n"
    "s_branch .B127_%=\n"
    ".R130_%=:\n"
    "s_nop 7\n"
    "s_mov_b32 s3, 0x30d40\n"
    ".Q133_%=:\n"
    "global_load_dwordx4 v[46:49], v16, s[14:15] offset:3072 sc1\n"
    "global_load_dwordx4 v[128:131], v16, s[20:21] offset:3072 sc1\n"
    "s_waitcnt vmcnt(0)\n"
    "v_add3_u32 v49, v49, v46, v47\n"
    "v_add3_u32 v49, v49, v48, s93\n"
    "v_add3_u32 v131, v131, v128, v129\n"
    "v_add3_u32 v131, v131, v130, s93\n"
    "v_or_b32 v3, v49, v131\n"
    "v_cmp_ne_u32 vcc, 0, v3\n"
    "s_cbranch_vccz .E132_%=\n"
    "s_sub_u32 s3, s3, 1\n"
    "s_cmp_lg_u32 s3, 0\n"
    "s_cbranch_scc1 .Q133_%=\n"
    ".E132_%=:\n"
    "s_nop 1\n"
    "s_branch .B131_%=\n"
    : "+{s[0:1]}"(ka) : "{s2}"(blockIdx.x), "{v0}"(threadIdx.x),
       "i"((__attribute__((address_space(3))) char*)smem)
    : "v1", "v2", "v3", "v4", "v5", "v6", "v7", "v8", "v9", "v10", "v11", "v12", "v13", "v14", "v15", "v16", "v17", "v18", "v19", "v20", "v21", "v22", "v23", "v24", "v25", "v26", "v27", "v28", "v29", "v30", "v31", "v32", "v33", "v34", "v35", "v36", "v37", "v38", "v39", "v40", "v41", "v42", "v43", "v44", "v45", "v46", "v47", "v48", "v49", "v50", "v51", "v52", "v53", "v54", "v55", "v56", "v57", "v58", "v59", "v60", "v61", "v62", "v63", "v64", "v65", "v66", "v67", "v68", "v69", "v70", "v71", "v72", "v73", "v74", "v75", "v76", "v77", "v78", "v79", "v80", "v81", "v82", "v83", "v84", "v85", "v86", "v87", "v88", "v89", "v90", "v91", "v92", "v93", "v94", "v95", "v96", "v97", "v98", "v99", "v100", "v101", "v102", "v103", "v104", "v105", "v106", "v107", "v108", "v109", "v110", "v111", "v112", "v113", "v114", "v115", "v116", "v117", "v118", "v119", "v120", "v121", "v122", "v123", "v124", "v125", "v126", "v127", "v128", "v129", "v130", "v131", "v132", "v133", "v134", "v135", "v136", "v137", "v138", "v139", "v140", "v141", "v142", "v143", "v144", "v145", "v146", "v147", "v148", "v149", "v150", "v151", "v152", "v153", "v154", "v155", "v156", "v157", "v158", "v159", "v160", "v161", "v162", "v163", "v164", "v165", "v166", "v167", "v168", "v169", "v170", "v171", "v172", "v173", "v174", "v175", "v176", "v177", "v178", "v179", "v180", "v181", "v182", "v183", "v184", "v185", "v186", "v187", "v188", "v189", "v190", "v191", "v192", "v193", "v194", "v195", "v196", "v197", "v198", "v199", "v200", "v201", "v202", "v203", "v204", "v205", "v206", "v207", "v208", "v209", "v210", "v211", "v212", "v213", "v214", "v215", "v216", "v217", "v218", "v219", "v220", "v221", "v222", "v223", "v224", "v225", "v226", "v227", "v228", "v229", "v230", "v231", "v232", "v233", "v234", "v235", "v236", "v237", "v238", "v239", "v240", "v241", "v242", "v243", "v244", "v245", "v246", "v247", "v248", "v249", "v250", "v251", "v252", "v253", "v254", "v255", "a0", "a1", "a2", "a3", "a4", "a5", "a6", "a7", "a8", "a9", "a10", "a11", "a12", "a13", "a14", "a15", "a16", "a17", "a18", "a19", "a20", "a21", "a22", "a23", "a24", "a25", "a26", "a27", "a28", "a29", "a30", "a31", "a32", "a33", "a34", "a35", "a36", "a37", "a38", "a39", "a40", "a41", "a42", "a43", "a44", "a45", "a46", "a47", "a48", "a49", "a50", "a51", "a52", "a53", "a54", "a55", "a56", "a57", "a58", "a59", "a60", "a61", "a62", "a63", "a64", "a65", "a66", "a67", "a68", "a69", "a70", "a71", "a72", "a73", "a74", "a75", "a76", "a77", "a78", "a79", "a80", "a81", "a82", "a83", "a84", "a85", "a86", "a87", "a88", "a89", "a90", "a91", "a92", "a93", "a94", "a95", "a96", "a97", "a98", "a99", "a100", "a101", "a102", "a103", "a104", "a105", "a106", "a107", "a108", "a109", "a110", "a111", "a112", "a113", "a114", "a115", "a116", "a117", "a118", "a119", "a120", "a121", "a122", "a123", "a124", "a125", "a126", "a127", "a128", "a129", "a130", "a131", "a132", "a133", "a134", "a135", "a136", "a137", "a138", "a139", "a140", "a141", "a142", "a143", "a144", "a145", "a146", "a147", "a148", "a149", "a150", "a151", "a152", "a153", "a154", "a155", "a156", "a157", "a158", "a159", "a160", "a161", "a162", "a163", "a164", "a165", "a166", "a167", "a168", "a169", "a170", "a171", "a172", "a173", "a174", "a175", "s3", "s4", "s5", "s6", "s7", "s8", "s9", "s10", "s11", "s12", "s13", "s14", "s15", "s16", "s17", "s18", "s19", "s20", "s21", "s22", "s23", "s24", "s25", "s26", "s27", "s28", "s29", "s30", "s31", "s34", "s35", "s36", "s37", "s38", "s39", "s40", "s41", "s42", "s43", "s44", "s45", "s46", "s47", "s48", "s49", "s50", "s51", "s52", "s53", "s54", "s55", "s56", "s57", "s58", "s59", "s60", "s61", "s62", "s63", "s64", "s65", "s66", "s67", "s68", "s69", "s70", "s71", "s72", "s73", "s74", "s75", "s76", "s77", "s78", "s79", "s80", "s81", "s82", "s83", "s84", "s85", "s86", "s87", "s88", "s89", "s90", "s91", "s92", "s93", "s94", "s95", "s96", "s97", "s98", "s99", "vcc", "scc", "memory");
}

extern "C" void kernel_launch(void* const* d_in, const int* in_sizes, int n_in, void* d_out, int out_size, void* d_ws, size_t ws_size,
                              hipStream_t stream) {
  fused<<<256, 256, 0, stream>>>(d_in[0], d_in[1], d_in[2], d_in[4], d_in[5], d_in[6], d_out, (char*)d_ws + 0);
}
```
